```python
import torch

def _softmax(x, axis=-1):
    return torch.softmax(x, dim=axis)

def _gelu(x):
    return torch.nn.functional.gelu(x, approximate='tanh')

def _silu(x):
    return torch.nn.functional.silu(x)

def _sigmoid(x):
    return torch.sigmoid(x)

def _layernorm(x, axis=-1, eps=1e-05):
    m = x.mean(dim=axis, keepdim=True)
    v = x.var(dim=axis, keepdim=True, unbiased=False)
    return (x - m) / torch.sqrt(v + eps)

def _scatter_add(tgt, idx, src, axis=0):
    return tgt.clone().index_add_(axis, idx, src)

def _segment_sum(src, seg, n):
    out = torch.zeros((n,) + src.shape[1:], dtype=src.dtype)
    return out.index_add_(0, seg, src)

def _pad(x, pw):
    flat = tuple((v for pair in reversed(pw) for v in pair))
    return torch.nn.functional.pad(x, flat)
kernel_name = 'adaLN_cond__grouped_kv_mqa__glu_gated_reglu__prompt'

def setup_inputs():

    _w = lambda a: (lambda t: t.to(torch.bfloat16) if t.is_floating_point() else t)(torch.as_tensor(a))

    def _A_setup():
        B, S, D, H = (1, 2048, 576, 32)
        Dh = D // H
        return {'x': _w((lambda *s: torch.randn(*s, dtype=torch.bfloat16) * 0.05)(B, S, D)), 'wq': _w((lambda *s: torch.randn(*s, dtype=torch.bfloat16) * 0.05)(D, H * Dh)), 'wk': _w((lambda *s: torch.randn(*s, dtype=torch.bfloat16) * 0.05)(D, Dh)), 'wv': _w((lambda *s: torch.randn(*s, dtype=torch.bfloat16) * 0.05)(D, Dh)), 'wo': _w((lambda *s: torch.randn(*s, dtype=torch.bfloat16) * 0.05)(H * Dh, D))}
    _A_in = _A_setup()

    def _B_setup():
        B, S, D, F = (1, 2048, 576, 4608)
        return {'x': _w((lambda *s: torch.randn(*s, dtype=torch.bfloat16) * 0.05)(B, S, D)), 'W_gate': _w((lambda *s: torch.randn(*s, dtype=torch.bfloat16) * 0.8)(D, F) * (2.0 / D) ** 0.5), 'W_up': _w((lambda *s: torch.randn(*s, dtype=torch.bfloat16) * 0.8)(D, F) * (2.0 / D) ** 0.5), 'W_down': _w((lambda *s: torch.randn(*s, dtype=torch.bfloat16) * 0.8)(F, D) * (1.0 / F) ** 0.5)}
    _B_in = _B_setup()
    return {'A_x': _A_in['x'], 'A_wq': _A_in['wq'], 'A_wk': _A_in['wk'], 'A_wv': _A_in['wv'], 'A_wo': _A_in['wo'], 'B_x': _B_in['x'], 'B_W_gate': _B_in['W_gate'], 'B_W_up': _B_in['W_up'], 'B_W_down': _B_in['W_down'], 'c': _w((lambda *s: torch.randn(*s, dtype=torch.bfloat16) * 0.2)(1, 576) * 8.0), 'w_mod': _w((lambda *s: torch.randn(*s, dtype=torch.bfloat16) * 0.2)(576, 6 * 576) * 576 ** (-0.5) * 8.0)}

def reference(A_x, A_wq, A_wk, A_wv, A_wo, B_x, B_W_gate, B_W_up, B_W_down, c, w_mod):

    def _A_ref(x, wq, wk, wv, wo):
        B, S, D = x.shape
        H = 32
        Dh = D // H
        q = (lambda x, *d: x.permute(*d))((x @ wq).reshape(B, S, H, Dh), 0, 2, 1, 3)
        k = x @ wk
        v = x @ wv
        scores = torch.einsum('bhqd,bkd->bhqk', q, k) * Dh ** (-0.5)
        p = _softmax(scores, axis=-1)
        o = torch.einsum('bhqk,bkd->bhqd', p, v)
        return (lambda x, *d: x.permute(*d))(o, 0, 2, 1, 3).reshape(B, S, H * Dh) @ wo

    def _B_ref(x, W_gate, W_up, W_down):
        gate = torch.einsum('bsd,df->bsf', x, W_gate)
        up = torch.einsum('bsd,df->bsf', x, W_up)
        gate = gate * (lambda x, dt: x.to(dt))(gate > 0, gate.dtype)
        h = gate * up
        return torch.einsum('bsf,fd->bsd', h, W_down)
    x = A_x
    mod = (_silu(c) @ w_mod).reshape(c.shape[0], 6, 1, -1)
    sa, ca, ga, sb, cb, gb = (mod[:, 0].clone(), mod[:, 1].clone(), mod[:, 2].clone(), mod[:, 3].clone(), mod[:, 4].clone(), mod[:, 5].clone())
    h = _layernorm(x, axis=-1) * (1 + ca) + sa
    x = x + ga * _A_ref(h, A_wq, A_wk, A_wv, A_wo)
    h = _layernorm(x, axis=-1) * (1 + cb) + sb
    x = x + gb * _B_ref(h, B_W_gate, B_W_up, B_W_down)
    return x
```

```cpp
#include <hip/hip_runtime.h>
#include <hip/hip_bf16.h>
typedef __hip_bfloat16 bf16;
__device__ inline float b2f(bf16 v){return __bfloat162float(v);}
constexpr int S=2048,D=576,H=32,DH=18,F=4608;

__global__ void k_mod(const bf16* c,const bf16* wm,float* mod){
  int j=blockIdx.x*64+threadIdx.x; float a=0;
  for(int k=0;k<D;k++){float cv=b2f(c[k]); float s=cv/(1.f+__expf(-cv)); a+=s*b2f(wm[k*6*D+j]);}
  mod[j]=a;
}
__global__ void k_ln(const float* xf,const bf16* xb,const float* shift,const float* scale,float* h){
  __shared__ float red[2][4];
  int r=blockIdx.x,t=threadIdx.x; float v[3]; float s=0;
  for(int i=0;i<3;i++){int c=t+i*192; v[i]= xf? xf[r*D+c]:b2f(xb[r*D+c]); s+=v[i];}
  for(int o=32;o;o>>=1) s+=__shfl_xor(s,o);
  if((t&63)==0) red[0][t>>6]=s; __syncthreads();
  float m=(red[0][0]+red[0][1]+red[0][2])/D; float q=0;
  for(int i=0;i<3;i++){float d=v[i]-m; q+=d*d;}
  for(int o=32;o;o>>=1) q+=__shfl_xor(q,o);
  if((t&63)==0) red[1][t>>6]=q; __syncthreads();
  float rs=rsqrtf((red[1][0]+red[1][1]+red[1][2])/D+1e-5f);
  for(int i=0;i<3;i++){int c=t+i*192; h[r*D+c]=(v[i]-m)*rs*(1.f+scale[c])+shift[c];}
}
__global__ void k_gemm(const float* A,const bf16* B,float* C,int M,int N,int K){
  __shared__ float a[16][17]; __shared__ float b[16][17];
  int tx=threadIdx.x,ty=threadIdx.y; int r=blockIdx.y*16+ty,c=blockIdx.x*16+tx; float acc=0;
  for(int k0=0;k0<K;k0+=16){
    a[ty][tx]=A[r*K+k0+tx]; b[ty][tx]= c<N? b2f(B[(k0+ty)*N+c]):0.f; __syncthreads();
    for(int k=0;k<16;k++) acc+=a[ty][k]*b[k][tx]; __syncthreads();
  }
  if(c<N) C[r*N+c]=acc;
}
__global__ void k_attn(const float* q,const float* k,const float* v,float* o){
  int idx=blockIdx.x*64+threadIdx.x; int tok=idx/H,h=idx%H; float qq[DH],acc[DH]; float m=-1e30f,l=0;
  for(int d=0;d<DH;d++){qq[d]=q[tok*D+h*DH+d]*0.23570226f; acc[d]=0;}
  for(int j=0;j<S;j++){ float s=0; for(int d=0;d<DH;d++) s+=qq[d]*k[j*DH+d];
    float mn=fmaxf(m,s); float a=__expf(m-mn),p=__expf(s-mn); l=l*a+p; for(int d=0;d<DH;d++) acc[d]=acc[d]*a+p*v[j*DH+d]; m=mn;}
  for(int d=0;d<DH;d++) o[tok*D+h*DH+d]=acc[d]/l;
}
__global__ void k_res1(const bf16* x,const float* g,const float* y,float* x1){int i=blockIdx.x*256+threadIdx.x; x1[i]=b2f(x[i])+g[i%D]*y[i];}
__global__ void k_glu(const float* g,const float* u,float* h){int i=blockIdx.x*256+threadIdx.x; h[i]=fmaxf(g[i],0.f)*u[i];}
__global__ void k_res2(const float* x1,const float* g,const float* y,bf16* out){int i=blockIdx.x*256+threadIdx.x; out[i]=__float2bfloat16(x1[i]+g[i%D]*y[i]);}

extern "C" void kernel_launch(void* const* d_in, const int* in_sizes, int n_in, void* d_out, int out_size, void* d_ws, size_t ws_size, hipStream_t stream){
  const bf16 *x=(const bf16*)d_in[0],*wq=(const bf16*)d_in[1],*wk=(const bf16*)d_in[2],*wv=(const bf16*)d_in[3],*wo=(const bf16*)d_in[4];
  const bf16 *wg=(const bf16*)d_in[6],*wu=(const bf16*)d_in[7],*wd=(const bf16*)d_in[8],*c=(const bf16*)d_in[9],*wm=(const bf16*)d_in[10];
  float* w=(float*)d_ws; float* mod=w; w+=4096; float* h=w; w+=S*D; float* q=w; w+=S*D; float* k=w; w+=S*32; float* v=w; w+=S*32; float* o=w; w+=S*D; float* y=w; w+=S*D; float* x1=w; w+=S*D;
  float* g=w; w+=S*F; float* u=w; w+=S*F; float* hh=w; w+=S*F;
  k_mod<<<54,64,0,stream>>>(c,wm,mod);
  k_ln<<<S,192,0,stream>>>(nullptr,x,mod,mod+D,h);
  dim3 b(16,16);
  k_gemm<<<dim3(36,128),b,0,stream>>>(h,wq,q,S,D,D);
  k_gemm<<<dim3(2,128),b,0,stream>>>(h,wk,k,S,DH,D);
  k_gemm<<<dim3(2,128),b,0,stream>>>(h,wv,v,S,DH,D);
  k_attn<<<S*H/64,64,0,stream>>>(q,k,v,o);
  k_gemm<<<dim3(36,128),b,0,stream>>>(o,wo,y,S,D,D);
  k_res1<<<S*D/256,256,0,stream>>>(x,mod+2*D,y,x1);
  k_ln<<<S,192,0,stream>>>(x1,nullptr,mod+3*D,mod+4*D,h);
  k_gemm<<<dim3(F/16,128),b,0,stream>>>(h,wg,g,S,F,D);
  k_gemm<<<dim3(F/16,128),b,0,stream>>>(h,wu,u,S,F,D);
  k_glu<<<S*F/256,256,0,stream>>>(g,u,hh);
  k_gemm<<<dim3(36,128),b,0,stream>>>(hh,wd,y,S,D,F);
  k_res2<<<S*D/256,256,0,stream>>>(x1,mod+5*D,y,(bf16*)d_out);
}
```

```cpp
#include <hip/hip_runtime.h>
#include <hip/hip_bf16.h>

typedef float    f32x16 __attribute__((ext_vector_type(16)));
typedef float    f32x4  __attribute__((ext_vector_type(4)));
typedef float    f32x2  __attribute__((ext_vector_type(2)));
typedef __bf16   bf16x8 __attribute__((ext_vector_type(8)));
typedef __bf16   bf16x2 __attribute__((ext_vector_type(2)));
typedef short    s16x4  __attribute__((ext_vector_type(4)));
typedef unsigned u32x4  __attribute__((ext_vector_type(4)));
typedef unsigned u32x2  __attribute__((ext_vector_type(2)));
typedef unsigned short u16;
typedef __amdgpu_buffer_rsrc_t rsrc_t;
#define DI __device__ __forceinline__

constexpr int S_ = 2048, D_ = 576, F_ = 4608;

constexpr size_t OFF_MODP = 0;
constexpr size_t OFF_MODF = 65536;
constexpr size_t OFF_XH   = 131072;
constexpr size_t OFF_WQKV = OFF_XH + 2359296;
constexpr size_t OFF_WO   = OFF_WQKV + 737280;
constexpr size_t OFF_QM   = OFF_WO + 663552;
constexpr size_t OFF_QT   = OFF_QM + 2097152;
constexpr size_t OFF_KV   = OFF_QT + 262144;
constexpr size_t OFF_OP   = OFF_KV + 155648;
constexpr size_t OFF_X1   = OFF_OP + 9437184;
constexpr size_t OFF_H2   = OFF_X1 + 2359296;
constexpr size_t OFF_W1   = OFF_H2 + 2359296;
constexpr size_t OFF_W2   = OFF_W1 + 10616832;
constexpr size_t OFF_HH   = OFF_W2 + 5308416;
constexpr size_t OFF_P2   = OFF_HH + 18874368;
constexpr size_t WS_END   = OFF_P2 + 9437184;
constexpr int KV_TILE = 2400, KV_BYTES = 64 * KV_TILE;

DI rsrc_t mk_rsrc(const void* p, unsigned bytes) { return __builtin_amdgcn_make_buffer_rsrc((void*)p, 0, bytes, 0x00020000); }
DI unsigned pk2(float lo, float hi) { f32x2 v = {lo, hi}; return __builtin_bit_cast(unsigned, __builtin_convertvector(v, bf16x2)); }
DI float blo(unsigned u) { return __uint_as_float(u << 16); }
DI float bhi(unsigned u) { return __uint_as_float(u & 0xffff0000u); }
DI int swap23(int r) { return (r & ~12) | ((r & 4) << 1) | ((r & 8) >> 1); }
DI int rfl(int x) { return __builtin_amdgcn_readfirstlane(x); }
#define MFMA32(a, b, c) __builtin_amdgcn_mfma_f32_32x32x16_bf16((a), (b), (c), 0, 0, 0)
#define MFMA16(a, b, c) __builtin_amdgcn_mfma_f32_16x16x32_bf16((a), (b), (c), 0, 0, 0)
#define BC8(x) __builtin_bit_cast(bf16x8, (x))

DI void dma16(rsrc_t rs, unsigned voff, unsigned soff, unsigned ldsaddr) {
  asm volatile("s_mov_b32 m0, %0\n\ts_nop 0\n\tbuffer_load_dwordx4 %1, %2, %3 offen lds" :: "s"(ldsaddr), "v"(voff), "s"(rs), "s"(soff) : "memory");
}
DI void dma16_lo(rsrc_t rs, unsigned voff, unsigned soff, unsigned ldsaddr) {
  asm volatile("s_mov_b32 m0, %0\n\ts_mov_b32 exec_hi, 0\n\tbuffer_load_dwordx4 %1, %2, %3 offen lds\n\ts_mov_b32 exec_hi, -1" :: "s"(ldsaddr), "v"(voff), "s"(rs), "s"(soff) : "memory");
}
template <int IMM> DI void ld16(u32x4& dst, rsrc_t rs, unsigned voff, unsigned soff) {
  asm volatile("buffer_load_dwordx4 %0, %1, %2, %3 offen offset:%4" : "=v"(dst) : "v"(voff), "s"(rs), "s"(soff), "i"(IMM) : "memory");
}
template <int N> DI void wait_vm() { asm volatile("s_waitcnt vmcnt(%0)" :: "i"(N) : "memory"); }
template <int N> DI void wait_vm_bar() { asm volatile("s_waitcnt vmcnt(%0) lgkmcnt(0)\n\ts_barrier" :: "i"(N) : "memory"); }
#define PIN(x) asm volatile("" : "+v"(x))

constexpr int K1_LN = 64, K1_MOD = 108, K1_TILE = 342, K1_GRID = K1_LN + K1_MOD + K1_TILE;
__global__ __launch_bounds__(256) void k_prep(const u16* __restrict__ x, const u16* __restrict__ wq, const u16* __restrict__ wk,
                                              const u16* __restrict__ wv, const u16* __restrict__ wo, const u16* __restrict__ c,
                                              const u16* __restrict__ wm, char* __restrict__ ws) {
  __shared__ float red[4][128];
  const int b = blockIdx.x, t = threadIdx.x, lane = t & 63, w = t >> 6;
  if (b < K1_LN) {
    const int tt = (b & 7) * 8 + (b >> 3);
    const int r = w * 8 + (lane >> 3), tok = tt * 32 + r, ci = lane & 7;
    u32x4 v[9];
#pragma unroll
    for (int i = 0; i < 9; ++i) v[i] = *(const u32x4*)(x + (size_t)tok * D_ + (ci + 8 * i) * 8);
    float s = 0.f;
#pragma unroll
    for (int i = 0; i < 9; ++i)
#pragma unroll
      for (int e = 0; e < 4; ++e) s += blo(v[i][e]) + bhi(v[i][e]);
    s += __shfl_xor(s, 1); s += __shfl_xor(s, 2); s += __shfl_xor(s, 4);
    const float mean = s * (1.f / D_);
    float q = 0.f;
#pragma unroll
    for (int i = 0; i < 9; ++i)
#pragma unroll
      for (int e = 0; e < 4; ++e) { float a = blo(v[i][e]) - mean, bb = bhi(v[i][e]) - mean; q += a * a + bb * bb; }
    q += __shfl_xor(q, 1); q += __shfl_xor(q, 2); q += __shfl_xor(q, 4);
    const float rs = rsqrtf(q * (1.f / D_) + 1e-5f);
    char* dst = ws + OFF_XH + (size_t)tt * 36 * 1024;
#pragma unroll
    for (int i = 0; i < 9; ++i) {
      const int c8 = ci + 8 * i;
      u32x4 o;
#pragma unroll
      for (int e = 0; e < 4; ++e) o[e] = pk2((blo(v[i][e]) - mean) * rs, (bhi(v[i][e]) - mean) * rs);
      *(u32x4*)(dst + (c8 >> 1) * 1024 + ((c8 & 1) * 32 + r) * 16) = o;
    }
  } else if (b < K1_LN + K1_MOD) {
    const int i = b - K1_LN, cg = i % 27, kq = i / 27;
    const int col = cg * 128 + (t & 15) * 8, r0 = kq * 144 + (t >> 4);
    u32x4 v[9]; float sc[9];
#pragma unroll
    for (int p = 0; p < 9; ++p) v[p] = *(const u32x4*)(wm + (size_t)(r0 + 16 * p) * (6 * D_) + col);
#pragma unroll
    for (int p = 0; p < 9; ++p) { float cv = __uint_as_float((unsigned)c[r0 + 16 * p] << 16); sc[p] = cv / (1.f + __expf(-cv)); }
    float a[8] = {0.f, 0.f, 0.f, 0.f, 0.f, 0.f, 0.f, 0.f};
#pragma unroll
    for (int p = 0; p < 9; ++p)
#pragma unroll
      for (int e = 0; e < 4; ++e) { a[2 * e] += sc[p] * blo(v[p][e]); a[2 * e + 1] += sc[p] * bhi(v[p][e]); }
#pragma unroll
    for (int e = 0; e < 8; ++e) { a[e] += __shfl_xor(a[e], 16); a[e] += __shfl_xor(a[e], 32); }
    if (lane < 16)
#pragma unroll
      for (int e = 0; e < 8; ++e) red[w][lane * 8 + e] = a[e];
    __syncthreads();
    if (t < 128) ((float*)(ws + OFF_MODP))[kq * 3456 + cg * 128 + t] = red[0][t] + red[1][t] + red[2][t] + red[3][t];
  } else {
    const int f = (b - K1_LN - K1_MOD) * 4 + w, r = lane & 31, h = lane >> 5;
    u16 e[8];
    char* dst;
    if (f < 720) {
      const int rt = f / 36, s = f % 36, k0 = 16 * s + 8 * h;
      const u16* src; int ld; bool ok = true;
      if (rt < 16)       { src = wq + (2 * rt + (r >> 4)) * 18 + (r & 15); ld = D_; }
      else if (rt < 18)  { src = wq + (16 * (rt - 16) + (r >> 1)) * 18 + 16 + (r & 1); ld = D_; }
      else               { ok = r < 18; src = (rt == 18 ? wk : wv) + (ok ? r : 0); ld = 18; }
#pragma unroll
      for (int j = 0; j < 8; ++j) { u16 val = src[(k0 + j) * ld]; e[j] = ok ? val : (u16)0; }
      dst = ws + OFF_WQKV + (size_t)f * 1024;
    } else {
      const int g2 = f - 720, g = g2 / 162, rt = (g2 % 162) / 9, ks = g2 % 9, col = 32 * rt + swap23(r);
#pragma unroll
      for (int j = 0; j < 8; ++j) {
        const int row = ks < 8 ? (8 * g + ks) * 18 + (j & 3) + 8 * (j >> 2) + 4 * h : (8 * g + 4 * h + (j >> 1)) * 18 + 16 + (j & 1);
        e[j] = wo[row * D_ + col];
      }
      dst = ws + OFF_WO + (size_t)g2 * 1024;
    }
    u32x4 o;
#pragma unroll
    for (int j = 0; j < 4; ++j) o[j] = (unsigned)e[2 * j] | ((unsigned)e[2 * j + 1] << 16);
    *(u32x4*)(dst + lane * 16) = o;
  }
}

DI int w1_pos(int i, int s, int gu) { const int u = i == 8 ? 0 : i + 1; return u < 3 ? s * 6 + u * 2 + gu : u * 36 + s * 2 + gu; }
DI void retile_w1(const u16* __restrict__ wg, const u16* __restrict__ wu, char* __restrict__ ws, int task, int lane) {
  const int cb = task % 36, k8 = (task / 36) % 72, gu = task / (36 * 72);
  const u16* src = (gu ? wu : wg) + (size_t)(k8 * 8) * F_ + cb * 128 + lane * 2;
  unsigned v[8];
#pragma unroll
  for (int j = 0; j < 8; ++j) v[j] = *(const unsigned*)(src + (size_t)j * F_);
  u32x4 lo, hi;
#pragma unroll
  for (int j = 0; j < 4; ++j) { lo[j] = __builtin_amdgcn_perm(v[2 * j + 1], v[2 * j], 0x05040100u); hi[j] = __builtin_amdgcn_perm(v[2 * j + 1], v[2 * j], 0x07060302u); }
  const int col = cb * 128 + lane * 2, n = col / 144, i = (col % 144) >> 4, r = col & 15;
  char* dst = ws + OFF_W1 + ((size_t)n * 324 + w1_pos(i, k8 >> 2, gu)) * 1024 + ((k8 & 3) * 16 + r) * 16;
  *(u32x4*)dst = lo; *(u32x4*)(dst + 16) = hi;
}
DI void retile_w2(const u16* __restrict__ wd, char* __restrict__ ws, int task, int lane) {
  const int cb = task % 5, q = (task / 5) & 3, st = task / 20;
  const int col = cb * 128 + lane * 2;
  if (col >= D_) return;
  const int pr = st / 9, sl = st % 9;
  unsigned v[8];
#pragma unroll
  for (int j = 0; j < 8; ++j) {
    const int feat = sl < 8 ? 144 * (2 * pr + (sl >> 2)) + 32 * (sl & 3) + 16 * (j >> 2) + 4 * q + (j & 3)
                            : 144 * (2 * pr + (j >> 2)) + 128 + 4 * q + (j & 3);
    v[j] = *(const unsigned*)(wd + (size_t)feat * D_ + col);
  }
  u32x4 lo, hi;
#pragma unroll
  for (int j = 0; j < 4; ++j) { lo[j] = __builtin_amdgcn_perm(v[2 * j + 1], v[2 * j], 0x05040100u); hi[j] = __builtin_amdgcn_perm(v[2 * j + 1], v[2 * j], 0x07060302u); }
  const int b = col / 144, cc = col % 144, a = st / 36, s = st % 36;
  int rt, r;
  if (cc < 128) { rt = 2 * (cc >> 5) + ((cc >> 2) & 1); r = ((cc >> 3) & 3) * 4 + (cc & 3); } else { rt = 8; r = cc - 128; }
  char* dst = ws + OFF_W2 + ((size_t)(a * 4 + b) * 324 + s * 9 + rt) * 1024 + (q * 16 + r) * 16;
  *(u32x4*)dst = lo; *(u32x4*)(dst + 16) = hi;
}
__global__ __launch_bounds__(256) void k_retile(const u16* __restrict__ wg, const u16* __restrict__ wu, const u16* __restrict__ wd, char* __restrict__ ws) {
  const int task = blockIdx.x * 4 + (threadIdx.x >> 6), lane = threadIdx.x & 63;
  if (task < 5184) retile_w1(wg, wu, ws, task, lane);
  else if (task < 5184 + 2880) retile_w2(wd, ws, task - 5184, lane);
}

__global__ __launch_bounds__(320) void k_qkv(char* __restrict__ ws) {
  __shared__ __attribute__((aligned(16))) char smem[36 * 1024 + 1152 * 4];
  float* sm = (float*)(smem + 36 * 1024);
  const int b = blockIdx.x, t = threadIdx.x, lane = t & 63, w = rfl(t >> 6);
  const int tt = (b & 7) * 8 + (b >> 5), cg = (b >> 3) & 3, rt = cg * 5 + w;
  const float* modp = (const float*)(ws + OFF_MODP);
  f32x4 mp[4];
  if (t < 288)
#pragma unroll
    for (int k = 0; k < 4; ++k) mp[k] = *(const f32x4*)(modp + k * 3456 + t * 4);
  u32x4 xv[8];
  const char* xh = ws + OFF_XH + (size_t)tt * 36 * 1024 + lane * 16;
#pragma unroll
  for (int i = 0; i < 8; ++i) { const int s = w + 5 * i; if (s < 36) xv[i] = *(const u32x4*)(xh + s * 1024); }
  u32x4 wf[36];
  const char* wsrc = ws + OFF_WQKV + (size_t)rt * 36 * 1024 + lane * 16;
#pragma unroll
  for (int s = 0; s < 36; ++s) wf[s] = *(const u32x4*)(wsrc + s * 1024);
  if (t < 288) { f32x4 m = mp[0] + mp[1] + mp[2] + mp[3]; if (t >= 144) m += 1.f; *(f32x4*)(sm + t * 4) = m; }
  if (b < 8 && t < 72) {
    const int idx = 1152 + b * 288 + t * 4;
    f32x4 m = *(const f32x4*)(modp + idx) + *(const f32x4*)(modp + 3456 + idx) + *(const f32x4*)(modp + 2 * 3456 + idx) + *(const f32x4*)(modp + 3 * 3456 + idx);
    *(f32x4*)((float*)(ws + OFF_MODF) + idx) = m;
  }
  __syncthreads();
#pragma unroll
  for (int i = 0; i < 8; ++i) {
    const int s = w + 5 * i;
    if (s < 36) {
      const int c0 = 16 * s + 8 * (lane >> 5);
      const f32x4 sh0 = *(const f32x4*)(sm + c0), sh1 = *(const f32x4*)(sm + c0 + 4), sc0 = *(const f32x4*)(sm + 576 + c0), sc1 = *(const f32x4*)(sm + 576 + c0 + 4);
      u32x4 o;
      o[0] = pk2(blo(xv[i][0]) * sc0[0] + sh0[0], bhi(xv[i][0]) * sc0[1] + sh0[1]);
      o[1] = pk2(blo(xv[i][1]) * sc0[2] + sh0[2], bhi(xv[i][1]) * sc0[3] + sh0[3]);
      o[2] = pk2(blo(xv[i][2]) * sc1[0] + sh1[0], bhi(xv[i][2]) * sc1[1] + sh1[1]);
      o[3] = pk2(blo(xv[i][3]) * sc1[2] + sh1[2], bhi(xv[i][3]) * sc1[3] + sh1[3]);
      *(u32x4*)(smem + s * 1024 + lane * 16) = o;
    }
  }
  __syncthreads();
  f32x16 acc = {};
  if (rt < 19) {
#pragma unroll
    for (int s = 0; s < 36; ++s) acc = MFMA32(BC8(wf[s]), *(const bf16x8*)(smem + s * 1024 + lane * 16), acc);
  } else {
#pragma unroll
    for (int s = 0; s < 36; ++s) acc = MFMA32(*(const bf16x8*)(smem + s * 1024 + lane * 16), BC8(wf[s]), acc);
  }
  const int r = lane & 31, h = lane >> 5;
  if (rt < 18) acc *= 0.34004506f;
  unsigned p[8];
#pragma unroll
  for (int i = 0; i < 8; ++i) p[i] = pk2(acc[2 * i], acc[2 * i + 1]);
  if (rt < 16) {
    char* dst = ws + OFF_QM + ((size_t)tt * 32 + 2 * rt) * 1024 + lane * 16;
    *(u32x4*)dst = u32x4{p[0], p[1], p[2], p[3]};
    *(u32x4*)(dst + 1024) = u32x4{p[4], p[5], p[6], p[7]};
  } else if (rt < 18) {
    char* dst = ws + OFF_QT + ((size_t)tt * 32 + 16 * (rt - 16) + 2 * h) * 128 + r * 4;
#pragma unroll
    for (int i = 0; i < 8; ++i) *(unsigned*)(dst + ((i & 1) + 4 * (i >> 1)) * 128) = p[i];
  } else if (rt == 18) {
    char* dst = ws + OFF_KV + tt * KV_TILE;
    *(u32x4*)(dst + lane * 16) = u32x4{p[0], p[1], p[2], p[3]};
    if (h == 0) *(unsigned*)(dst + 1024 + r * 4) = p[4];
  } else {
    char* dst = ws + OFF_KV + tt * KV_TILE + 1152;
    u32x4 v0 = {p[0], p[1], p[2], p[3]}, v1 = {p[4], p[5], p[6], p[7]};
    if (r == 18) { v0 = u32x4{0x3f803f80u, 0x3f803f80u, 0x3f803f80u, 0x3f803f80u}; v1 = v0; }
    if (r == 19) { v0 = u32x4{0u, 0u, 0u, 0u}; v1 = v0; }
    if (r < 19 || lane == 51) {
      *(u32x4*)(dst + (h * 19 + r) * 16) = v0;
      *(u32x4*)(dst + 624 + (h * 19 + r) * 16) = v1;
    }
  }
}

struct AttnRes { f32x16 o; };
DI float half_swap_max(float m) {
  auto pr = __builtin_amdgcn_permlane32_swap(__float_as_uint(m), __float_as_uint(m), false, false);
  return fmaxf(__uint_as_float(pr[0]), __uint_as_float(pr[1]));
}
DI f32x16 qk_tile(const char* kp, const char* ktp, bf16x8 qm, s16x4 qt, f32x16 c) {
  const bf16x8 km = *(const bf16x8*)kp;
  u32x2 kt2 = {*(const unsigned*)ktp, 0u};
  f32x16 s = __builtin_amdgcn_mfma_f32_32x32x8bf16_1k(__builtin_bit_cast(s16x4, kt2), qt, c, 0, 0, 0);
  return MFMA32(km, qm, s);
}
DI float max16(const f32x16& s) {
  float m = fmaxf(fmaxf(s[0], s[1]), fmaxf(s[2], s[3]));
#pragma unroll
  for (int i = 4; i < 16; i += 2) m = fmaxf(m, fmaxf(s[i], s[i + 1]));
  return m;
}

__global__ __launch_bounds__(512, 2) void k_attn(char* __restrict__ ws) {
  extern __shared__ __attribute__((aligned(16))) char smem[];
  const int b = blockIdx.x, t = threadIdx.x, lane = t & 63, w = rfl(t >> 6);
  const int tt = (b & 7) * 8 + (b >> 5), g = (b >> 3) & 3, head = g * 8 + w;
  const int r = lane & 31, h = lane >> 5;
  const unsigned lds0 = (unsigned)(size_t)smem;
  const unsigned l16 = lane * 16;
  const rsrc_t rq = mk_rsrc(ws + OFF_QM, 2097152 + 262144), rkv = mk_rsrc(ws + OFF_KV, KV_BYTES), rwo = mk_rsrc(ws + OFF_WO, 663552);
  u32x4 qm4; unsigned qt1;
  ld16<0>(qm4, rq, l16, (unsigned)((tt * 32 + head) * 1024));
  asm volatile("buffer_load_dword %0, %1, %2, %3 offen" : "=v"(qt1) : "v"((unsigned)(r * 4)), "s"(rq), "s"((unsigned)(2097152 + (tt * 32 + head) * 128)) : "memory");
#pragma unroll
  for (int i = 0; i < 19; ++i) { const int pc = (i * 8 + w) < 150 ? (i * 8 + w) : 149; dma16(rkv, l16, (unsigned)(pc * 1024), lds0 + pc * 1024); }
  u32x4 wf[27];
  {
    const unsigned s0 = (unsigned)((g * 18 + w) * 9 * 1024), s1 = s0 + 8 * 9 * 1024, s2 = (unsigned)((g * 18 + 16 + (w & 1)) * 9 * 1024);
#define LDW(i, so) ld16<0>(wf[i + 0], rwo, l16, so); ld16<1024>(wf[i + 1], rwo, l16, so); ld16<2048>(wf[i + 2], rwo, l16, so); ld16<3072>(wf[i + 3], rwo, l16, so); \
                   ld16<0>(wf[i + 4], rwo, l16, so + 4096); ld16<1024>(wf[i + 5], rwo, l16, so + 4096); ld16<2048>(wf[i + 6], rwo, l16, so + 4096); ld16<3072>(wf[i + 7], rwo, l16, so + 4096); \
                   ld16<0>(wf[i + 8], rwo, l16, so + 8192);
    LDW(0, s0) LDW(9, s1) LDW(18, s2)
  }
  asm volatile("s_waitcnt vmcnt(41)\n\ts_barrier" : "+v"(qm4), "+v"(qt1) :: "memory");
  const bf16x8 qm = BC8(qm4);
  const u32x2 qt2 = {h ? 0u : qt1, 0u};
  const s16x4 qt = __builtin_bit_cast(s16x4, qt2);
  const char* kp = smem + lane * 16;
  const char* ktp = smem + 1024 + r * 4;
  const char* vp = smem + 1152 + (r < 19 ? (h * 19 + r) * 16 : 38 * 16);
  float m;
  { f32x16 z = {}; m = half_swap_max(max16(qk_tile(kp, ktp, qm, qt, z))); }
  f32x16 o;
  float l;
  for (int attempt = 0;; ++attempt) {
    f32x16 negm;
#pragma unroll
    for (int i = 0; i < 16; ++i) negm[i] = -m;
    o = f32x16{};
    f32x16 sc = qk_tile(kp, ktp, qm, qt, negm);
    for (int c = 0; c < 4; ++c) {
      if (attempt == 0) {
        if (c == 1) wait_vm_bar<36>();
        if (c == 2) wait_vm_bar<31>();
        if (c == 3) wait_vm_bar<27>();
      }
#pragma unroll 2
      for (int i = 0; i < 16; ++i) {
        const int tl = c * 16 + i, tn = tl < 63 ? tl + 1 : 63;
        const bf16x8 v0 = *(const bf16x8*)(vp + tl * KV_TILE), v1 = *(const bf16x8*)(vp + tl * KV_TILE + 624);
        f32x16 sn = qk_tile(kp + tn * KV_TILE, ktp + tn * KV_TILE, qm, qt, negm);
        u32x4 p0, p1;
#pragma unroll
        for (int e = 0; e < 4; ++e) p0[e] = pk2(__builtin_amdgcn_exp2f(sc[2 * e]), __builtin_amdgcn_exp2f(sc[2 * e + 1]));
        o = MFMA32(v0, BC8(p0), o);
#pragma unroll
        for (int e = 0; e < 4; ++e) p1[e] = pk2(__builtin_amdgcn_exp2f(sc[8 + 2 * e]), __builtin_amdgcn_exp2f(sc[8 + 2 * e + 1]));
        o = MFMA32(v1, BC8(p1), o);
        sc = sn;
      }
    }
    { auto pr = __builtin_amdgcn_permlane32_swap(__float_as_uint(o[10]), __float_as_uint(o[10]), false, false); l = __uint_as_float(pr[0]); }
    float am = fabsf(o[0]);
#pragma unroll
    for (int i = 1; i < 10; ++i) am = fmaxf(am, fabsf(o[i]));
    const bool ok = (l < 1.8446744e19f) && (am < 3.0e38f) && (o[0] + o[1] + o[2] + o[3] + o[4] + o[5] + o[6] + o[7] + o[8] + o[9] == o[0] + o[1] + o[2] + o[3] + o[4] + o[5] + o[6] + o[7] + o[8] + o[9]);
    if (attempt == 1 || __all(ok)) break;
    float mm = -3.4e38f;
    for (int tl = 0; tl < 64; ++tl) { f32x16 z = {}; mm = fmaxf(mm, max16(qk_tile(kp + tl * KV_TILE, ktp + tl * KV_TILE, qm, qt, z))); }
    m = half_swap_max(mm);
  }
  const float inv = 1.f / l;
  char* ox = smem + KV_BYTES;
  *(u32x4*)(ox + w * 1024 + lane * 16) = u32x4{pk2(o[0] * inv, o[1] * inv), pk2(o[2] * inv, o[3] * inv), pk2(o[4] * inv, o[5] * inv), pk2(o[6] * inv, o[7] * inv)};
  if (h == 0) *(unsigned*)(ox + 8192 + ((w >> 2) * 32 + r) * 16 + (w & 3) * 4) = pk2(o[8] * inv, o[9] * inv);
  asm volatile("s_waitcnt vmcnt(0) lgkmcnt(0)\n\ts_barrier" : "+v"(wf[0]), "+v"(wf[1]), "+v"(wf[2]), "+v"(wf[3]), "+v"(wf[4]), "+v"(wf[5]), "+v"(wf[6]), "+v"(wf[7]), "+v"(wf[8]),
               "+v"(wf[9]), "+v"(wf[10]), "+v"(wf[11]), "+v"(wf[12]), "+v"(wf[13]), "+v"(wf[14]), "+v"(wf[15]), "+v"(wf[16]), "+v"(wf[17]),
               "+v"(wf[18]), "+v"(wf[19]), "+v"(wf[20]), "+v"(wf[21]), "+v"(wf[22]), "+v"(wf[23]), "+v"(wf[24]), "+v"(wf[25]), "+v"(wf[26]) :: "memory");
  bf16x8 of[9];
#pragma unroll
  for (int k = 0; k < 9; ++k) of[k] = *(const bf16x8*)(ox + k * 1024 + lane * 16);
  char* dst = ws + OFF_OP + (size_t)(tt * 4 + g) * 36 * 1024 + lane * 16;
#pragma unroll
  for (int j = 0; j < 3; ++j) {
    if (j == 2 && w >= 2) break;
    f32x16 acc = {};
#pragma unroll
    for (int k = 0; k < 9; ++k) acc = MFMA32(BC8(wf[9 * j + k]), of[k], acc);
    const int rt = j < 2 ? w + 8 * j : 16 + w;
    *(u32x4*)(dst + rt * 2048) = u32x4{pk2(acc[0], acc[1]), pk2(acc[2], acc[3]), pk2(acc[4], acc[5]), pk2(acc[6], acc[7])};
    *(u32x4*)(dst + rt * 2048 + 1024) = u32x4{pk2(acc[8], acc[9]), pk2(acc[10], acc[11]), pk2(acc[12], acc[13]), pk2(acc[14], acc[15])};
  }
}

__global__ __launch_bounds__(576) void k_ln2(const u16* __restrict__ x, char* __restrict__ ws) {
  __shared__ float red[2][9][8];
  const int b = blockIdx.x, t = threadIdx.x, lane = t & 63, w = t >> 6;
  const int tt = (b & 7) * 8 + (b >> 5), tq = (b >> 3) & 3;
  const int r = tq * 8 + (lane & 7), tok = tt * 32 + r, c8 = t >> 3;
  const char* pp = ws + OFF_OP + (size_t)tt * 4 * 36 * 1024 + (c8 >> 1) * 1024 + ((c8 & 1) * 32 + r) * 16;
  u32x4 pv[4];
#pragma unroll
  for (int g = 0; g < 4; ++g) pv[g] = *(const u32x4*)(pp + g * 36 * 1024);
  const u32x4 xv = *(const u32x4*)(x + (size_t)tok * D_ + c8 * 8);
  const float* mf = (const float*)(ws + OFF_MODF);
  float ga[8], sb[8], cb[8];
#pragma unroll
  for (int e = 0; e < 2; ++e) {
    const f32x4 a = *(const f32x4*)(mf + 2 * D_ + c8 * 8 + 4 * e), s = *(const f32x4*)(mf + 3 * D_ + c8 * 8 + 4 * e), cc = *(const f32x4*)(mf + 4 * D_ + c8 * 8 + 4 * e);
#pragma unroll
    for (int i = 0; i < 4; ++i) { ga[4 * e + i] = a[i]; sb[4 * e + i] = s[i]; cb[4 * e + i] = cc[i] + 1.f; }
  }
  float v[8];
#pragma unroll
  for (int e = 0; e < 4; ++e) {
    v[2 * e]     = blo(xv[e]) + ga[2 * e] * (blo(pv[0][e]) + blo(pv[1][e]) + blo(pv[2][e]) + blo(pv[3][e]));
    v[2 * e + 1] = bhi(xv[e]) + ga[2 * e + 1] * (bhi(pv[0][e]) + bhi(pv[1][e]) + bhi(pv[2][e]) + bhi(pv[3][e]));
  }
  u32x4 xo = {pk2(v[0], v[1]), pk2(v[2], v[3]), pk2(v[4], v[5]), pk2(v[6], v[7])};
  *(u32x4*)(ws + OFF_X1 + ((size_t)tok * D_ + c8 * 8) * 2) = xo;
  float s = ((v[0] + v[1]) + (v[2] + v[3])) + ((v[4] + v[5]) + (v[6] + v[7]));
  s += __shfl_xor(s, 8); s += __shfl_xor(s, 16); s += __shfl_xor(s, 32);
  if (lane < 8) red[0][w][lane] = s;
  __syncthreads();
  float mean = 0.f;
#pragma unroll
  for (int i = 0; i < 9; ++i) mean += red[0][i][lane & 7];
  mean *= (1.f / D_);
  float q = 0.f;
#pragma unroll
  for (int e = 0; e < 8; ++e) { v[e] -= mean; q += v[e] * v[e]; }
  q += __shfl_xor(q, 8); q += __shfl_xor(q, 16); q += __shfl_xor(q, 32);
  if (lane < 8) red[1][w][lane] = q;
  __syncthreads();
  float var = 0.f;
#pragma unroll
  for (int i = 0; i < 9; ++i) var += red[1][i][lane & 7];
  const float rs = rsqrtf(var * (1.f / D_) + 1e-5f);
  u32x4 ho;
#pragma unroll
  for (int e = 0; e < 4; ++e) ho[e] = pk2(v[2 * e] * rs * cb[2 * e] + sb[2 * e], v[2 * e + 1] * rs * cb[2 * e + 1] + sb[2 * e + 1]);
  *(u32x4*)(ws + OFF_H2 + ((size_t)(tok >> 4) * 18 + (c8 >> 2)) * 1024 + ((c8 & 3) * 16 + (tok & 15)) * 16) = ho;
}

DI void dma_stage(rsrc_t rw, unsigned l16, unsigned soff, unsigned ldsb, int w, int lane) {
#pragma unroll
  for (int i = 0; i < 4; ++i) dma16(rw, l16, soff + (i * 8 + w) * 1024, ldsb + (i * 8 + w) * 1024);
  dma16_lo(rw, l16, soff + 32768 + w * 512, ldsb + 32768 + w * 512);
}
DI u32x2 glu4(const f32x4& g, const f32x4& u) {
  return u32x2{pk2(fmaxf(g[0], 0.f) * u[0], fmaxf(g[1], 0.f) * u[1]), pk2(fmaxf(g[2], 0.f) * u[2], fmaxf(g[3], 0.f) * u[3])};
}
__global__ __launch_bounds__(512, 2) void k_gemm1(char* __restrict__ ws) {
  extern __shared__ __attribute__((aligned(16))) char smem[];
  const int b = blockIdx.x, t = threadIdx.x, lane = t & 63, w = rfl(t >> 6);
  const int xcd = b & 7, idx = b >> 3;
  const int mt = (xcd & 1) * 4 + (idx & 3), n = (xcd >> 1) * 8 + (idx >> 2);
  const unsigned lds0 = (unsigned)(size_t)smem, l16 = lane * 16;
  const rsrc_t rx = mk_rsrc(ws + OFF_H2, 2359296), rw = mk_rsrc(ws + OFF_W1 + (size_t)n * 324 * 1024, 324 * 1024), rh = mk_rsrc(ws + OFF_HH, 18874368);
  const int t16 = mt * 16 + w * 2;
  u32x4 xf[2][18];
  const unsigned xs0 = (unsigned)(t16 * 18 * 1024), xs1 = xs0 + 18 * 1024;
#define LDX(s) ld16<((s) & 3) * 1024>(xf[0][s], rx, l16, xs0 + ((s) >> 2) * 4096); ld16<((s) & 3) * 1024>(xf[1][s], rx, l16, xs1 + ((s) >> 2) * 4096);
  dma_stage(rw, l16, 0, lds0, w, lane);
  LDX(0) LDX(1) LDX(2) LDX(3) LDX(4) LDX(5)
  dma_stage(rw, l16, 36864, lds0 + 36864, w, lane);
  LDX(6) LDX(7) LDX(8) LDX(9) LDX(10) LDX(11)
  dma_stage(rw, l16, 2 * 36864, lds0 + 2 * 36864, w, lane);
  LDX(12) LDX(13) LDX(14) LDX(15) LDX(16) LDX(17)
  dma_stage(rw, l16, 3 * 36864, lds0 + 3 * 36864, w, lane);
  const char* lp = smem + lane * 16;
  const unsigned hbase = (unsigned)(t16 * 144 * 1024) + (unsigned)((n >> 1) * 9 * 1024);
  f32x4 acc[3][2][2] = {};
#define WAITX(N, s) asm volatile("s_waitcnt vmcnt(" #N ")" : "+v"(xf[0][s]), "+v"(xf[1][s]) :: "memory");
#define STEP0(s) { _Pragma("unroll") for (int u = 0; u < 3; ++u) _Pragma("unroll") for (int gu = 0; gu < 2; ++gu) { \
      const bf16x8 a = *(const bf16x8*)(lp + ((s) * 6 + u * 2 + gu) * 1024); \
      acc[u][gu][0] = MFMA16(a, BC8(xf[0][s]), acc[u][gu][0]); acc[u][gu][1] = MFMA16(a, BC8(xf[1][s]), acc[u][gu][1]); } }
  wait_vm_bar<51>();
  WAITX(49, 0) STEP0(0) WAITX(47, 1) STEP0(1) WAITX(45, 2) STEP0(2) WAITX(43, 3) STEP0(3) WAITX(41, 4) STEP0(4) WAITX(39, 5) STEP0(5)
  wait_vm_bar<34>();
  dma_stage(rw, l16, 4 * 36864, lds0, w, lane);
  WAITX(37, 6) STEP0(6) WAITX(35, 7) STEP0(7) WAITX(33, 8) STEP0(8) WAITX(31, 9) STEP0(9) WAITX(29, 10) STEP0(10) WAITX(27, 11) STEP0(11)
  wait_vm_bar<22>();
  dma_stage(rw, l16, 5 * 36864, lds0 + 36864, w, lane);
  WAITX(25, 12) STEP0(12) WAITX(23, 13) STEP0(13) WAITX(21, 14) STEP0(14) WAITX(19, 15) STEP0(15) WAITX(17, 16) STEP0(16) WAITX(15, 17) STEP0(17)
  wait_vm_bar<10>();
  dma_stage(rw, l16, 6 * 36864, lds0 + 2 * 36864, w, lane);
#pragma unroll
  for (int tk = 0; tk < 2; ++tk) {
    const u32x2 a = glu4(acc[0][0][tk], acc[0][1][tk]), p0 = glu4(acc[1][0][tk], acc[1][1][tk]), p1 = glu4(acc[2][0][tk], acc[2][1][tk]);
    __builtin_amdgcn_raw_buffer_store_b64(a, rh, l16 + (n & 1) * 8, hbase + tk * 147456 + 8 * 1024, 16);
    __builtin_amdgcn_raw_buffer_store_b128(u32x4{p0[0], p0[1], p1[0], p1[1]}, rh, l16, hbase + tk * 147456 + (n & 1) * 4096, 16);
  }
  u32x2 keep[2];
#pragma unroll
  for (int g = 3; g < 9; ++g) {
    const char* sp = lp + (g & 3) * 36864;
    f32x4 c[2][2] = {};
#pragma unroll
    for (int s = 0; s < 18; ++s)
#pragma unroll
      for (int gu = 0; gu < 2; ++gu) {
        const bf16x8 a = *(const bf16x8*)(sp + (s * 2 + gu) * 1024);
        c[gu][0] = MFMA16(a, BC8(xf[0][s]), c[gu][0]); c[gu][1] = MFMA16(a, BC8(xf[1][s]), c[gu][1]);
      }
    if (g == 3) { wait_vm_bar<14>(); dma_stage(rw, l16, 7 * 36864, lds0 + 3 * 36864, w, lane); }
    if (g == 4) { wait_vm_bar<14>(); dma_stage(rw, l16, 8 * 36864, lds0, w, lane); }
    if (g == 5) wait_vm_bar<16>();
    if (g == 6) wait_vm_bar<7>();
    if (g == 7) wait_vm_bar<4>();
#pragma unroll
    for (int tk = 0; tk < 2; ++tk) {
      const u32x2 p = glu4(c[0][tk], c[1][tk]);
      if (g & 1) keep[tk] = p;
      else __builtin_amdgcn_raw_buffer_store_b128(u32x4{keep[tk][0], keep[tk][1], p[0], p[1]}, rh, l16, hbase + tk * 147456 + ((n & 1) * 4 + (g - 2) / 2) * 1024, 16);
    }
  }
}

__global__ __launch_bounds__(512, 2) void k_gemm2(char* __restrict__ ws) {
  extern __shared__ __attribute__((aligned(16))) char smem[];
  const int b = blockIdx.x, t = threadIdx.x, lane = t & 63, w = rfl(t >> 6);
  const int xcd = b & 7, idx = b >> 3;
  const int a = xcd >> 1, m = (xcd & 1) * 8 + (idx >> 2), bb = idx & 3;
  const int tg = w & 3, par = w >> 2;
  const unsigned lds0 = (unsigned)(size_t)smem, l16 = lane * 16;
  const rsrc_t rw = mk_rsrc(ws + OFF_W2 + (size_t)(a * 4 + bb) * 324 * 1024, 324 * 1024), rh = mk_rsrc(ws + OFF_HH, 18874368);
  const int t16 = m * 8 + tg * 2;
  const unsigned hs0 = (unsigned)((t16 * 144 + a * 36 + par * 2) * 1024), hs1 = hs0 + 147456;
  u32x4 hf[9][2][2];
#define LDH(g) ld16<0>(hf[g][0][0], rh, l16, hs0 + (g) * 4096); ld16<0>(hf[g][0][1], rh, l16, hs1 + (g) * 4096); \
               ld16<1024>(hf[g][1][0], rh, l16, hs0 + (g) * 4096); ld16<1024>(hf[g][1][1], rh, l16, hs1 + (g) * 4096);
  dma_stage(rw, l16, 0, lds0, w, lane); LDH(0)
  dma_stage(rw, l16, 36864, lds0 + 36864, w, lane); LDH(1)
  dma_stage(rw, l16, 2 * 36864, lds0 + 2 * 36864, w, lane); LDH(2)
  dma_stage(rw, l16, 3 * 36864, lds0 + 3 * 36864, w, lane); LDH(3)
  const char* lp = smem + lane * 16 + par * 18 * 1024;
  f32x4 acc[9][2] = {};
#define WAITH(N, g) asm volatile("s_waitcnt vmcnt(" #N ") lgkmcnt(0)\n\ts_barrier" : "+v"(hf[g][0][0]), "+v"(hf[g][0][1]), "+v"(hf[g][1][0]), "+v"(hf[g][1][1]) :: "memory");
#define STAGE2(g) { const char* sp = lp + ((g) & 3) * 36864; _Pragma("unroll") for (int s = 0; s < 2; ++s) _Pragma("unroll") for (int rt = 0; rt < 9; ++rt) { \
      const bf16x8 wv = *(const bf16x8*)(sp + (s * 9 + rt) * 1024); \
      acc[rt][0] = MFMA16(wv, BC8(hf[g][s][0]), acc[rt][0]); acc[rt][1] = MFMA16(wv, BC8(hf[g][s][1]), acc[rt][1]); } }
#define NEXT2(g) dma_stage(rw, l16, (g) * 36864, lds0 + ((g) & 3) * 36864, w, lane); LDH(g)
  WAITH(27, 0) STAGE2(0)
  WAITH(18, 1) NEXT2(4) STAGE2(1)
  WAITH(18, 2) NEXT2(5) STAGE2(2)
  WAITH(18, 3) NEXT2(6) STAGE2(3)
  WAITH(18, 4) NEXT2(7) STAGE2(4)
  WAITH(18, 5) NEXT2(8) STAGE2(5)
  WAITH(18, 6) STAGE2(6)
  WAITH(9, 7) STAGE2(7)
  WAITH(0, 8) STAGE2(8)
  asm volatile("s_waitcnt lgkmcnt(0)\n\ts_barrier" ::: "memory");
  char* ex = smem + (size_t)w * 9216 + lane * 16;
#pragma unroll
  for (int rt = 0; rt < 9; ++rt) *(f32x4*)(ex + rt * 1024) = par ? acc[rt][0] : acc[rt][1];
  asm volatile("s_waitcnt lgkmcnt(0)\n\ts_barrier" ::: "memory");
  const char* ey = smem + (size_t)(w ^ 4) * 9216 + lane * 16;
  u32x2 pk[9];
#pragma unroll
  for (int rt = 0; rt < 9; ++rt) {
    const f32x4 v = (par ? acc[rt][1] : acc[rt][0]) + *(const f32x4*)(ey + rt * 1024);
    pk[rt] = u32x2{pk2(v[0], v[1]), pk2(v[2], v[3])};
  }
  char* dst = ws + OFF_P2 + ((size_t)((m * 4 + bb) * 4 + a) * 8 + tg * 2 + par) * 4608;
#pragma unroll
  for (int e = 0; e < 4; ++e) *(u32x4*)(dst + e * 1024 + lane * 16) = u32x4{pk[2 * e][0], pk[2 * e][1], pk[2 * e + 1][0], pk[2 * e + 1][1]};
  *(u32x2*)(dst + 4096 + lane * 8) = pk[8];
}

__global__ __launch_bounds__(320) void k_final(char* __restrict__ ws, u16* __restrict__ out) {
  const int t = threadIdx.x, lane = t & 63, e = t >> 6;
  const int it = blockIdx.x, tl = it & 7, bb = (it >> 3) & 3, m = it >> 5;
  const int tok = m * 128 + tl * 16 + (lane & 15), q = lane >> 4;
  const char* pp = ws + OFF_P2 + ((size_t)((m * 4 + bb) * 4) * 8 + tl) * 4608;
  const float* gb = (const float*)(ws + OFF_MODF) + 5 * D_;
  if (e < 4) {
    const int ch = bb * 144 + 32 * e + 8 * q;
    u32x4 pv[4];
#pragma unroll
    for (int a = 0; a < 4; ++a) pv[a] = *(const u32x4*)(pp + (size_t)a * 8 * 4608 + e * 1024 + lane * 16);
    const u32x4 xv = *(const u32x4*)(ws + OFF_X1 + ((size_t)tok * D_ + ch) * 2);
    const f32x4 g0 = *(const f32x4*)(gb + ch), g1 = *(const f32x4*)(gb + ch + 4);
    u32x4 o;
#pragma unroll
    for (int i = 0; i < 4; ++i) {
      const float gl = i < 2 ? g0[2 * i] : g1[2 * i - 4], gh = i < 2 ? g0[2 * i + 1] : g1[2 * i - 3];
      o[i] = pk2(blo(xv[i]) + gl * (blo(pv[0][i]) + blo(pv[1][i]) + blo(pv[2][i]) + blo(pv[3][i])),
                 bhi(xv[i]) + gh * (bhi(pv[0][i]) + bhi(pv[1][i]) + bhi(pv[2][i]) + bhi(pv[3][i])));
    }
    *(u32x4*)(out + (size_t)tok * D_ + ch) = o;
  } else {
    const int ch = bb * 144 + 128 + 4 * q;
    u32x2 pv[4];
#pragma unroll
    for (int a = 0; a < 4; ++a) pv[a] = *(const u32x2*)(pp + (size_t)a * 8 * 4608 + 4096 + lane * 8);
    const u32x2 xv = *(const u32x2*)(ws + OFF_X1 + ((size_t)tok * D_ + ch) * 2);
    const f32x4 g0 = *(const f32x4*)(gb + ch);
    u32x2 o;
#pragma unroll
    for (int i = 0; i < 2; ++i)
      o[i] = pk2(blo(xv[i]) + g0[2 * i] * (blo(pv[0][i]) + blo(pv[1][i]) + blo(pv[2][i]) + blo(pv[3][i])),
                 bhi(xv[i]) + g0[2 * i + 1] * (bhi(pv[0][i]) + bhi(pv[1][i]) + bhi(pv[2][i]) + bhi(pv[3][i])));
    *(u32x2*)(out + (size_t)tok * D_ + ch) = o;
  }
}

extern "C" void kernel_launch(void* const* d_in, const int* in_sizes, int n_in, void* d_out, int out_size, void* d_ws, size_t ws_size,
                              hipStream_t stream) {
  const u16 *x = (const u16*)d_in[0], *wq = (const u16*)d_in[1], *wk = (const u16*)d_in[2], *wv = (const u16*)d_in[3], *wo = (const u16*)d_in[4];
  const u16 *wg = (const u16*)d_in[6], *wu = (const u16*)d_in[7], *wd = (const u16*)d_in[8], *c = (const u16*)d_in[9], *wm = (const u16*)d_in[10];
  char* ws = (char*)d_ws;
  (void)hipFuncSetAttribute((const void*)k_attn, hipFuncAttributeMaxDynamicSharedMemorySize, KV_BYTES + 9216);
  (void)hipFuncSetAttribute((const void*)k_gemm1, hipFuncAttributeMaxDynamicSharedMemorySize, 147456);
  (void)hipFuncSetAttribute((const void*)k_gemm2, hipFuncAttributeMaxDynamicSharedMemorySize, 147456);
  k_retile<<<2016, 256, 0, stream>>>(wg, wu, wd, ws);
  k_prep<<<K1_GRID, 256, 0, stream>>>(x, wq, wk, wv, wo, c, wm, ws);
  k_qkv<<<256, 320, 0, stream>>>(ws);
  k_attn<<<256, 512, KV_BYTES + 9216, stream>>>(ws);
  k_ln2<<<256, 576, 0, stream>>>(x, ws);
  k_gemm1<<<256, 512, 147456, stream>>>(ws);
  k_gemm2<<<256, 512, 147456, stream>>>(ws);
  k_final<<<512, 320, 0, stream>>>(ws, (u16*)d_out);
}
```

```cpp
#include <hip/hip_runtime.h>
#include <hip/hip_bf16.h>

typedef float    f32x16 __attribute__((ext_vector_type(16)));
typedef float    f32x4  __attribute__((ext_vector_type(4)));
typedef float    f32x2  __attribute__((ext_vector_type(2)));
typedef __bf16   bf16x8 __attribute__((ext_vector_type(8)));
typedef __bf16   bf16x2 __attribute__((ext_vector_type(2)));
typedef short    s16x4  __attribute__((ext_vector_type(4)));
typedef unsigned u32x4  __attribute__((ext_vector_type(4)));
typedef unsigned u32x2  __attribute__((ext_vector_type(2)));
typedef unsigned short u16;
typedef __amdgpu_buffer_rsrc_t rsrc_t;
#define DI __device__ __forceinline__

constexpr int S_ = 2048, D_ = 576, F_ = 4608;

constexpr size_t OFF_MODP = 0;
constexpr size_t OFF_MODF = 65536;
constexpr size_t OFF_CNT  = 98304;
constexpr size_t OFF_CNT2 = OFF_CNT + 16384;
constexpr size_t OFF_CNT3 = OFF_CNT2 + 32768;
constexpr size_t OFF_CNT4 = OFF_CNT3 + 32768;
constexpr size_t OFF_XH   = 262144;
constexpr size_t OFF_WQKV = OFF_XH + 2359296;
constexpr size_t OFF_WO   = OFF_WQKV + 737280;
constexpr size_t OFF_QM   = OFF_WO + 663552;
constexpr size_t OFF_QT   = OFF_QM + 2097152;
constexpr size_t OFF_KV   = OFF_QT + 262144;
constexpr size_t OFF_OP   = OFF_KV + 155648;
constexpr size_t OFF_X1   = OFF_OP + 9437184;
constexpr size_t OFF_H2   = OFF_X1 + 2359296;
constexpr size_t OFF_W1   = OFF_H2 + 2359296;
constexpr size_t OFF_W2   = OFF_W1 + 10616832;
constexpr size_t OFF_HH   = OFF_W2 + 5308416;
constexpr size_t OFF_P2   = OFF_HH + 18874368;
constexpr size_t WS_END   = OFF_P2 + 9437184;
constexpr int KV_TILE = 2400, KV_BYTES = 64 * KV_TILE;

DI rsrc_t mk_rsrc(const void* p, unsigned bytes) { return __builtin_amdgcn_make_buffer_rsrc((void*)p, 0, bytes, 0x00020000); }
DI unsigned pk2(float lo, float hi) { f32x2 v = {lo, hi}; return __builtin_bit_cast(unsigned, __builtin_convertvector(v, bf16x2)); }
DI float blo(unsigned u) { return __uint_as_float(u << 16); }
DI float bhi(unsigned u) { return __uint_as_float(u & 0xffff0000u); }
DI void st16_wt(void* p, u32x4 v) { asm volatile("global_store_dwordx4 %0, %1, off sc1\n\ts_nop 1" :: "v"(p), "v"(v) : "memory"); }
DI int swap23(int r) { return (r & ~12) | ((r & 4) << 1) | ((r & 8) >> 1); }
DI int rfl(int x) { return __builtin_amdgcn_readfirstlane(x); }
#define MFMA32(a, b, c) __builtin_amdgcn_mfma_f32_32x32x16_bf16((a), (b), (c), 0, 0, 0)
#define BC8(x) __builtin_bit_cast(bf16x8, (x))

DI void dma16(rsrc_t rs, unsigned voff, unsigned soff, unsigned ldsaddr) {
  asm volatile("s_mov_b32 m0, %0\n\ts_nop 0\n\tbuffer_load_dwordx4 %1, %2, %3 offen lds" :: "s"(ldsaddr), "v"(voff), "s"(rs), "s"(soff) : "memory");
}
DI void dma16s(rsrc_t rs, unsigned voff, unsigned soff, unsigned ldsaddr) {
  asm volatile("s_mov_b32 m0, %0\n\ts_nop 0\n\tbuffer_load_dwordx4 %1, %2, %3 offen sc1 lds" :: "s"(ldsaddr), "v"(voff), "s"(rs), "s"(soff) : "memory");
}
template <int IMM> DI void ld16(u32x4& dst, rsrc_t rs, unsigned voff, unsigned soff) {
  asm volatile("buffer_load_dwordx4 %0, %1, %2, %3 offen offset:%4" : "=v"(dst) : "v"(voff), "s"(rs), "s"(soff), "i"(IMM) : "memory");
}
typedef __attribute__((address_space(1))) unsigned gu32;
DI void wait_count(char* cnt, unsigned target) {
  for (unsigned spin = 0; spin < (1u << 21); ++spin) {
    if (__hip_atomic_load((gu32*)cnt, __ATOMIC_RELAXED, __HIP_MEMORY_SCOPE_AGENT) >= target) break;
    __builtin_amdgcn_s_sleep(1);
  }
}
DI u32x4 gather8(float v) {
  const unsigned p = pk2(v, __uint_as_float(__builtin_amdgcn_update_dpp(0u, __float_as_uint(v), 0x128, 0xf, 0xf, false)));
  const auto a = __builtin_amdgcn_permlane16_swap(p, p, false, false);
  const auto c0 = __builtin_amdgcn_permlane32_swap(a[0], a[0], false, false), c1 = __builtin_amdgcn_permlane32_swap(a[1], a[1], false, false);
  return u32x4{c0[0], c1[0], c0[1], c1[1]};
}
DI float xsum8_16_32(float v) {
  v += __uint_as_float(__builtin_amdgcn_update_dpp(0u, __float_as_uint(v), 0x128, 0xf, 0xf, false));
  auto a = __builtin_amdgcn_permlane16_swap(__float_as_uint(v), __float_as_uint(v), false, false);
  v = __uint_as_float(a[0]) + __uint_as_float(a[1]);
  auto c = __builtin_amdgcn_permlane32_swap(__float_as_uint(v), __float_as_uint(v), false, false);
  return __uint_as_float(c[0]) + __uint_as_float(c[1]);
}
#define CODE_RSRC(rs, from, to) rsrc_t rs; { unsigned long long p_; unsigned n_; \
  asm volatile("s_getpc_b64 s[60:61]\n\ts_add_u32 s60, s60, " from "@rel32@lo+4\n\ts_addc_u32 s61, s61, " from "@rel32@hi+12\n\ts_mov_b32 %1, " to "-" from \
               : "={s[60:61]}"(p_), "=s"(n_) :: "scc"); rs = mk_rsrc((const void*)p_, n_); }

DI float dpp_add(float v, const int ctrl) {
  switch (ctrl) {
    case 0xb1:  return v + __uint_as_float(__builtin_amdgcn_update_dpp(0u, __float_as_uint(v), 0xb1, 0xf, 0xf, false));
    case 0x4e:  return v + __uint_as_float(__builtin_amdgcn_update_dpp(0u, __float_as_uint(v), 0x4e, 0xf, 0xf, false));
    case 0x124: return v + __uint_as_float(__builtin_amdgcn_update_dpp(0u, __float_as_uint(v), 0x124, 0xf, 0xf, false));
    default:    return v + __uint_as_float(__builtin_amdgcn_update_dpp(0u, __float_as_uint(v), 0x128, 0xf, 0xf, false));
  }
}
DI float sum32(float v) {
  v = dpp_add(dpp_add(dpp_add(dpp_add(v, 0xb1), 0x4e), 0x124), 0x128);
  auto a = __builtin_amdgcn_permlane16_swap(__float_as_uint(v), __float_as_uint(v), false, false);
  return __uint_as_float(a[0]) + __uint_as_float(a[1]);
}
extern "C" __global__ __launch_bounds__(896) void k_prep(const u16* __restrict__ x, const u16* __restrict__ wq, const u16* __restrict__ wk,
                                              const u16* __restrict__ wv, const u16* __restrict__ wo, const u16* __restrict__ c,
                                              const u16* __restrict__ wm, char* __restrict__ ws) {
  __shared__ float red[4][64];
  __shared__ unsigned ticket;
  const int b = blockIdx.x, t = threadIdx.x, lane = t & 63, w = rfl(t >> 6);
  if (t == 0) ticket = 0;
  asm volatile("" :: "s"(x), "s"(wq), "s"(wk), "s"(wv), "s"(wo), "s"(c), "s"(wm), "s"(ws));
  if (w < 4) {
    const int tt = (b & 7) * 8 + (b >> 5);
    const int r = ((b >> 3) & 3) * 8 + w * 2 + (lane >> 5), tok = tt * 32 + r, ci = lane & 31;
    const bool on = ci < 24;
    const rsrc_t rx = mk_rsrc(x, S_ * D_ * 2), rxh = mk_rsrc(ws + OFF_XH, 2359296);
    u32x4 v[3];
#pragma unroll
    for (int i = 0; i < 3; ++i) v[i] = __builtin_amdgcn_raw_buffer_load_b128(rx, on ? (unsigned)(tok * (D_ * 2) + ci * 16) : 0x80000000u, i * 384, 2);
    asm volatile("s_waitcnt lgkmcnt(0)\n\ts_barrier" ::: "memory");
    float f[24], s = 0.f;
#pragma unroll
    for (int i = 0; i < 3; ++i)
#pragma unroll
      for (int e = 0; e < 4; ++e) { f[i * 8 + 2 * e] = blo(v[i][e]); f[i * 8 + 2 * e + 1] = bhi(v[i][e]); s += f[i * 8 + 2 * e] + f[i * 8 + 2 * e + 1]; }
    const float mean = sum32(s) * (1.f / D_);
    float q = 0.f;
#pragma unroll
    for (int i = 0; i < 24; ++i) { f[i] -= mean; q += f[i] * f[i]; }
    const float rs = rsqrtf(sum32(on ? q : 0.f) * (1.f / D_) + 1e-5f);
#pragma unroll
    for (int i = 0; i < 3; ++i) {
      const u32x4 o = {pk2(f[i * 8] * rs, f[i * 8 + 1] * rs), pk2(f[i * 8 + 2] * rs, f[i * 8 + 3] * rs), pk2(f[i * 8 + 4] * rs, f[i * 8 + 5] * rs), pk2(f[i * 8 + 6] * rs, f[i * 8 + 7] * rs)};
      __builtin_amdgcn_raw_buffer_store_b128(o, rxh, (on ? (unsigned)(tt * 36864 + ci * 512 + r * 16) : 0x80000000u) + i * 12288, 0, 0);
    }
    if (w == 0 && lane < 4 && (lane || b < 128)) *(unsigned*)(ws + (lane == 0 ? OFF_CNT : OFF_CNT2 + (lane - 1) * 32768) + b * 128) = 0u;
  } else if (w < 8) {
    const int bm = b < 216 ? b : 0, cg = bm % 54, kq = bm / 54;
    const int r0 = kq * 144 + (w - 4) * 36 + (lane >> 3);
    const rsrc_t rm = mk_rsrc(wm, D_ * 6 * D_ * 2);
    const unsigned voff = (unsigned)(r0 * (6 * D_ * 2) + cg * 128 + (lane & 7) * 16);
    u32x4 v[5]; float sc[5];
#pragma unroll
    for (int p = 0; p < 5; ++p) v[p] = __builtin_amdgcn_raw_buffer_load_b128(rm, (p == 4 && lane >= 32) || b >= 216 ? 0x80000000u : voff, p * 8 * (6 * D_ * 2), 2);
    const rsrc_t rcv = mk_rsrc(c, D_ * 2);
    unsigned cr[5];
#pragma unroll
    for (int p = 0; p < 5; ++p) cr[p] = __builtin_amdgcn_raw_buffer_load_b16(rcv, r0 * 2 + p * 16, 0, 0);
    asm volatile("s_waitcnt lgkmcnt(0)\n\ts_barrier" ::: "memory");
#pragma unroll
    for (int p = 0; p < 5; ++p) { const float cv = __uint_as_float(cr[p] << 16); sc[p] = cv * __builtin_amdgcn_rcpf(1.f + __expf(-cv)); }
    if (lane >= 32) sc[4] = 0.f;
    float a[8] = {0.f, 0.f, 0.f, 0.f, 0.f, 0.f, 0.f, 0.f};
#pragma unroll
    for (int p = 0; p < 5; ++p)
#pragma unroll
      for (int e = 0; e < 4; ++e) { a[2 * e] += sc[p] * blo(v[p][e]); a[2 * e + 1] += sc[p] * bhi(v[p][e]); }
#pragma unroll
    for (int e = 0; e < 8; ++e) a[e] = xsum8_16_32(a[e]);
    if (lane < 8) { *(f32x4*)&red[w - 4][lane * 8] = f32x4{a[0], a[1], a[2], a[3]}; *(f32x4*)&red[w - 4][lane * 8 + 4] = f32x4{a[4], a[5], a[6], a[7]}; }
    asm volatile("s_waitcnt lgkmcnt(0)" ::: "memory");
    unsigned tk = 0;
    if (lane == 0) tk = __hip_atomic_fetch_add(&ticket, 1u, __ATOMIC_RELAXED, __HIP_MEMORY_SCOPE_WORKGROUP);
    if (rfl(tk) == 3 && b < 216) {
      asm volatile("" ::: "memory");
      ((float*)(ws + OFF_MODP))[kq * 3456 + cg * 64 + lane] = (red[0][lane] + red[1][lane]) + (red[2][lane] + red[3][lane]);
    }
  } else {
    const int f = (w - 8) * 256 + b, r = lane & 31, h = lane >> 5;
    CODE_RSRC(rc, "k_qkv", "k_text_end");
    const u32x4 cz = __builtin_amdgcn_raw_buffer_load_b128(rc, w >= 11 ? lane * 16 + ((w - 11) * 32 + (b >> 3)) * 1024 : 0x80000000u, 0, 0);
    const bool isq = f < 720;
    const int g2 = f - 720, rt = isq ? f / 36 : (g2 % 162) / 9, ks = isq ? f % 36 : g2 % 9, gq = g2 / 162;
    const bool kv = isq && rt >= 18, tail = !isq && ks == 8;
    const unsigned ld = kv ? 36 : D_ * 2;
    const int base = isq ? 16 * ks : tail ? 144 * gq + 16 : (8 * gq + ks) * 18, rb = tail ? 18 : 2, rcs = isq ? 4 : tail ? 36 : 8, rh = isq ? 8 : tail ? 72 : 4;
    const int col = !isq ? 32 * rt + swap23(r) : rt < 16 ? (2 * rt + (r >> 4)) * 18 + (r & 15) : rt < 18 ? (16 * (rt - 16) + (r >> 1)) * 18 + 16 + (r & 1) : r;
    const rsrc_t rs = mk_rsrc(!isq ? wo : rt < 18 ? wq : rt == 18 ? wk : wv, kv ? D_ * 36 : D_ * D_ * 2);
    const unsigned vo = (f < 1368 && !(kv && r >= 18)) ? col * 2 + h * rh * ld : 0x80000000u;
    unsigned e[8];
#pragma unroll
    for (int j = 0; j < 8; ++j) e[j] = __builtin_amdgcn_raw_buffer_load_b16(rs, vo, (base + (j & 1) + ((j >> 1) & 1) * rb + (j >> 2) * rcs) * ld, 0);
    asm volatile("s_waitcnt lgkmcnt(0)\n\ts_barrier" ::: "memory");
    const rsrc_t rd = mk_rsrc(ws + OFF_WQKV, 1368 * 1024);
    __builtin_amdgcn_raw_buffer_store_b128(u32x4{e[0] | (e[1] << 16), e[2] | (e[3] << 16), e[4] | (e[5] << 16), e[6] | (e[7] << 16)}, rd, f * 1024 + lane * 16, 0, 0);
    asm volatile("" :: "v"(cz));
  }
}

DI int w1_pos(int i, int s, int gu) {
  const int u = i == 8 ? 0 : i + 1;
  if (u < 4) return s < 15 ? s * 8 + u * 2 + gu : 120 + u * 6 + (s - 15) * 2 + gu;
  if (u < 7) return u * 36 + s * 2 + gu;
  return s < 15 ? 252 + s * 4 + (u - 7) * 2 + gu : 312 + (u - 7) * 6 + (s - 15) * 2 + gu;
}
DI int w2_pos(int d, int sl) {
  if (sl >= 7) return 126 + 2 * d + sl - 7;
  if (d < 3) return sl < 4 ? 4 * d + sl : sl < 6 ? 8 + 2 * d + sl : 18 + d;
  if (d < 14) return 7 * d + sl;
  return sl == 6 ? 98 + d - 14 : 102 + 8 * (sl >> 1) + 2 * (d - 14) + (sl & 1);
}
struct RetileTask { unsigned srow, ld, jump, vsrc, vlo, vhi; };
DI RetileTask retile_task(int task, int lane) {
  RetileTask t;
  if (task < 1296) {
    const int cb = task % 36, s = (task / 36) % 18, gu = task / 648;
    t.ld = F_ * 2; t.jump = 0; t.srow = (unsigned)(s * 32 * F_ * 2 + cb * 256); t.vsrc = lane * 4;
    const int col = cb * 128 + lane * 2, n = col / 144, cc = col - n * 144;
    const bool sg = cc >= 128;
    const int row = sg ? (cc - 128) >> 1 : (cc & 31) >> 1, i = sg ? 8 : 2 * (cc >> 5);
    t.vlo = (unsigned)((n * 324 + w1_pos(i, s, gu)) * 1024 + row * 16);
    t.vhi = sg ? t.vlo + 128 : (unsigned)((n * 324 + w1_pos(i + 1, s, gu)) * 1024 + row * 16);
  } else {
    const int tk = task - 1296, cb = tk % 5, st = tk / 5, aq = st / 36, ps = st % 36;
    t.ld = D_ * 2;
    const bool sh = ps >= 8 && ps < 12;
    const int pp = ps < 8 ? ps : ps - 4;
    const int row0 = sh ? 288 * (4 * aq + ps - 8) + 128 : 144 * (8 * aq + (pp & 7)) + 32 * (pp >> 3);
    t.jump = sh ? 128 * D_ * 2 : 0; t.srow = (unsigned)(row0 * D_ * 2 + cb * 256);
    const int col = cb * 128 + lane * 2, bq = col / 144, cc = col - bq * 144;
    const bool sg = cc >= 128, ok = col < D_ && task < 2016;
    const int row = sg ? (cc - 128) >> 1 : (cc & 31) >> 1, rel = ((cc >> 5) - aq - 1) & 3, sl = sg ? 6 : rel == 3 ? 7 : 2 * rel;
    const unsigned lo = (unsigned)(10616832 + ((aq * 4 + bq) * 324 + 2 * w2_pos(ps >> 1, sl) + (ps & 1)) * 1024 + row * 16);
    t.vsrc = ok ? lane * 4 : 0x80000000u; t.vlo = ok ? lo : 0x80000000u; t.vhi = ok ? lo + (sg ? 128 : 2048) : 0x80000000u;
  }
  return t;
}

constexpr int K2_PRE = 12;
template <bool SWAP> DI f32x16 qkv_mma(rsrc_t rw, const char* smem, int lane) {
  u32x4 wf[36], hb[36];
  asm volatile("" ::: "memory");
#pragma unroll
  for (int s = 0; s < K2_PRE; ++s) wf[s] = __builtin_amdgcn_raw_buffer_load_b128(rw, lane * 16, s * 1024, SWAP ? 1 : 0);
  f32x16 acc = {};
#pragma unroll
  for (int s = 0; s < 36; ++s) {
    if (s % 12 == 0) {
      asm volatile("s_barrier" ::: "memory");
#pragma unroll
      for (int k = 0; k < 6; ++k) hb[s + k] = *(const u32x4*)(smem + (s + k) * 1024 + lane * 16);
    }
    if (s + K2_PRE < 36) wf[s + K2_PRE] = __builtin_amdgcn_raw_buffer_load_b128(rw, lane * 16, (s + K2_PRE) * 1024, 0);
    acc = SWAP ? MFMA32(BC8(hb[s]), BC8(wf[s]), acc) : MFMA32(BC8(wf[s]), BC8(hb[s]), acc);
    if (s % 12 < 6) hb[s + 6] = *(const u32x4*)(smem + (s + 6) * 1024 + lane * 16);
    __builtin_amdgcn_sched_barrier(0);
  }
  return acc;
}
extern "C" __global__ __launch_bounds__(704) __attribute__((amdgpu_waves_per_eu(3, 3))) void k_qkv(char* __restrict__ ws) {
  __shared__ __attribute__((aligned(16))) char smem[36 * 1024 + 1152 * 4];
  const int b = blockIdx.x, t = threadIdx.x, lane = t & 63, w = rfl(t >> 6);
  const int tt = (b & 7) * 8 + (b >> 5), cg = (b >> 3) & 3, rt = cg * 5 + w - 6;
  if (w < 6) {
    const float* modp = (const float*)(ws + OFF_MODP);
    float* sm = (float*)(smem + 36 * 1024) + w * 192;
    const int l48 = lane < 48 ? lane : 0, l24 = l48 >= 24 ? l48 - 24 : l48;
    const int mo = (l48 >= 24 ? 576 : 0) + (l24 >> 3) * 192 + w * 32 + (l24 & 7) * 4;
    f32x4 mp[4];
#pragma unroll
    for (int k = 0; k < 4; ++k) mp[k] = *(const f32x4*)(modp + k * 3456 + mo);
    u32x4 xv[6];
    const char* xh = ws + OFF_XH + ((size_t)tt * 36 + w * 2) * 1024 + lane * 16;
#pragma unroll
    for (int i = 0; i < 6; ++i) xv[i] = *(const u32x4*)(xh + ((i >> 1) * 12 + (i & 1)) * 1024);
    if (lane < 48) *(f32x4*)(sm + lane * 4) = (mp[0] + mp[1]) + (mp[2] + mp[3]) + (lane >= 24 ? 1.f : 0.f);
    asm volatile("s_waitcnt lgkmcnt(0)" ::: "memory");
#pragma unroll
    for (int i = 0; i < 6; ++i) {
      const int c0 = 16 * i + 8 * (lane >> 5);
      const f32x4 sh0 = *(const f32x4*)(sm + c0), sh1 = *(const f32x4*)(sm + c0 + 4), sc0 = *(const f32x4*)(sm + 96 + c0), sc1 = *(const f32x4*)(sm + 96 + c0 + 4);
      u32x4 o;
      o[0] = pk2(blo(xv[i][0]) * sc0[0] + sh0[0], bhi(xv[i][0]) * sc0[1] + sh0[1]);
      o[1] = pk2(blo(xv[i][1]) * sc0[2] + sh0[2], bhi(xv[i][1]) * sc0[3] + sh0[3]);
      o[2] = pk2(blo(xv[i][2]) * sc1[0] + sh1[0], bhi(xv[i][2]) * sc1[1] + sh1[1]);
      o[3] = pk2(blo(xv[i][3]) * sc1[2] + sh1[2], bhi(xv[i][3]) * sc1[3] + sh1[3]);
      *(u32x4*)(smem + ((i >> 1) * 12 + w * 2 + (i & 1)) * 1024 + lane * 16) = o;
      if (i & 1) asm volatile("s_waitcnt lgkmcnt(0)\n\ts_barrier" ::: "memory");
    }
    if (b < 3 && w < 3) {
      const int idx = 1152 + ((b * 3 + w) * 64 + lane) * 4;
      *(f32x4*)((float*)(ws + OFF_MODF) + idx) = *(const f32x4*)(modp + idx) + *(const f32x4*)(modp + 3456 + idx) + *(const f32x4*)(modp + 2 * 3456 + idx) + *(const f32x4*)(modp + 3 * 3456 + idx);
    }
    return;
  }
  const rsrc_t rw = mk_rsrc(ws + OFF_WQKV + (size_t)rt * 36 * 1024, 36 * 1024);
  __builtin_amdgcn_s_sleep(6);
  f32x16 acc;
  if (rt < 19) acc = qkv_mma<false>(rw, smem, lane); else acc = qkv_mma<true>(rw, smem, lane);
  const int r = lane & 31, h = lane >> 5;
  if (rt < 18) acc *= 0.34004506f;
  unsigned p[8];
#pragma unroll
  for (int i = 0; i < 8; ++i) p[i] = pk2(acc[2 * i], acc[2 * i + 1]);
  if (rt < 16) {
    char* dst = ws + OFF_QM + ((size_t)tt * 32 + 2 * rt) * 1024 + lane * 16;
    st16_wt(dst, u32x4{p[0], p[1], p[2], p[3]});
    st16_wt(dst + 1024, u32x4{p[4], p[5], p[6], p[7]});
  } else if (rt < 18) {
    char* dst = ws + OFF_QT + ((size_t)tt * 32 + 16 * (rt - 16) + 2 * h) * 128 + r * 4;
#pragma unroll
    for (int i = 0; i < 8; ++i) *(unsigned*)(dst + ((i & 1) + 4 * (i >> 1)) * 128) = p[i];
  } else if (rt == 18) {
    char* dst = ws + OFF_KV + tt * KV_TILE;
    *(u32x4*)(dst + lane * 16) = u32x4{p[0], p[1], p[2], p[3]};
    if (h == 0) *(unsigned*)(dst + 1024 + r * 4) = p[4];
  } else {
    char* dst = ws + OFF_KV + tt * KV_TILE + 1152;
    u32x4 v0 = {p[0], p[1], p[2], p[3]}, v1 = {p[4], p[5], p[6], p[7]};
    if (r == 18) { v0 = u32x4{0x3f803f80u, 0x3f803f80u, 0x3f803f80u, 0x3f803f80u}; v1 = v0; }
    if (r == 19) { v0 = u32x4{0u, 0u, 0u, 0u}; v1 = v0; }
    if (r < 19 || lane == 51) {
      *(u32x4*)(dst + (h * 19 + r) * 16) = v0;
      *(u32x4*)(dst + 624 + (h * 19 + r) * 16) = v1;
    }
  }
}

DI float half_swap_max(float m) {
  auto pr = __builtin_amdgcn_permlane32_swap(__float_as_uint(m), __float_as_uint(m), false, false);
  return fmaxf(__uint_as_float(pr[0]), __uint_as_float(pr[1]));
}
DI f32x16 qk_tile(const char* kp, const char* ktp, bf16x8 qm, s16x4 qt, f32x16 c) {
  const bf16x8 km = *(const bf16x8*)kp;
  u32x2 kt2 = {*(const unsigned*)ktp, 0u};
  f32x16 s = __builtin_amdgcn_mfma_f32_32x32x8bf16_1k(__builtin_bit_cast(s16x4, kt2), qt, c, 0, 0, 0);
  return MFMA32(km, qm, s);
}
DI float max16(const f32x16& s) {
  float m = fmaxf(fmaxf(s[0], s[1]), fmaxf(s[2], s[3]));
#pragma unroll
  for (int i = 4; i < 16; i += 2) m = fmaxf(m, fmaxf(s[i], s[i + 1]));
  return m;
}

#define ATTN_ASM \
  "s_setprio 3\nv_exp_f32 v96, v32\nv_exp_f32 v104, v33\nv_exp_f32 v97, v34\nv_exp_f32 v105, v35\nv_cvt_pk_bf16_f32 v96, v96, v104\n" \
  "v_cvt_pk_bf16_f32 v97, v97, v105\ns_add_u32 m0, s41, 24576\ns_add_u32 s47, s40, 24576\nbuffer_load_dwordx4 v133, %[rkv], s47 offen lds\ns_add_u32 m0, s41, 32768\ns_add_u32 s47, s40, 32768\n" \
  "buffer_load_dwordx4 v133, %[rkv], s47 offen lds\nv_exp_f32 v98, v36\nv_exp_f32 v104, v37\nv_exp_f32 v99, v38\nv_exp_f32 v105, v39\nv_cvt_pk_bf16_f32 v98, v98, v104\n" \
  "v_cvt_pk_bf16_f32 v99, v99, v105\nds_read_b128 v[114:117], v132 offset:624\nv_exp_f32 v100, v40\nv_exp_f32 v104, v41\nv_exp_f32 v101, v42\nv_exp_f32 v105, v43\n" \
  "v_cvt_pk_bf16_f32 v100, v100, v104\nv_cvt_pk_bf16_f32 v101, v101, v105\ns_waitcnt lgkmcnt(1)\nv_mfma_f32_32x32x16_bf16 v[0:15], v[110:113], v[96:99], 0\nv_exp_f32 v102, v44\nv_exp_f32 v104, v45\n" \
  "v_exp_f32 v103, v46\nv_exp_f32 v105, v47\nv_cvt_pk_bf16_f32 v102, v102, v104\nv_cvt_pk_bf16_f32 v103, v103, v105\nv_mfma_f32_32x32x16_bf16 v[64:79], v[106:109], v[118:121], v[64:79]\ns_add_u32 m0, s41, 40960\n" \
  "s_add_u32 s47, s40, 40960\nbuffer_load_dwordx4 v133, %[rkv], s47 offen lds\ns_add_u32 m0, s41, 49152\ns_add_u32 s47, s40, 49152\nbuffer_load_dwordx4 v133, %[rkv], s47 offen lds\nv_exp_f32 v96, v48\n" \
  "v_exp_f32 v104, v49\nv_exp_f32 v97, v50\nv_exp_f32 v105, v51\nv_cvt_pk_bf16_f32 v96, v96, v104\nv_cvt_pk_bf16_f32 v97, v97, v105\ns_waitcnt lgkmcnt(0)\n" \
  "v_mfma_f32_32x32x16_bf16 v[0:15], v[114:117], v[100:103], v[0:15]\ns_add_u32 m0, s41, 57344\ns_add_u32 s47, s40, 57344\nbuffer_load_dwordx4 v133, %[rkv], s47 offen lds\ns_add_u32 m0, s41, 65536\ns_add_u32 s47, s40, 65536\n" \
  "buffer_load_dwordx4 v133, %[rkv], s47 offen lds\nv_exp_f32 v98, v52\nv_exp_f32 v104, v53\nv_exp_f32 v99, v54\nv_exp_f32 v105, v55\nv_cvt_pk_bf16_f32 v98, v98, v104\n" \
  "v_cvt_pk_bf16_f32 v99, v99, v105\nv_mfma_f32_32x32x16_bf16 v[80:95], v[106:109], v[122:125], v[80:95]\nds_read_b128 v[106:109], v130 offset:9600\nv_exp_f32 v100, v56\nv_exp_f32 v104, v57\nv_exp_f32 v101, v58\n" \
  "v_exp_f32 v105, v59\nv_cvt_pk_bf16_f32 v100, v100, v104\nv_cvt_pk_bf16_f32 v101, v101, v105\nv_mfma_f32_32x32x16_bf16 v[16:31], v[110:113], v[96:99], 0\nds_read_b128 v[110:113], v132 offset:4800\ns_add_u32 m0, s41, 73728\n" \
  "s_add_u32 s47, s40, 73728\nbuffer_load_dwordx4 v133, %[rkv], s47 offen lds\ns_add_u32 m0, s41, 81920\ns_add_u32 s47, s40, 81920\nbuffer_load_dwordx4 v133, %[rkv], s47 offen lds\nv_exp_f32 v102, v60\n" \
  "v_exp_f32 v104, v61\nv_exp_f32 v103, v62\nv_exp_f32 v105, v63\nv_cvt_pk_bf16_f32 v102, v102, v104\nv_cvt_pk_bf16_f32 v103, v103, v105\nv_mfma_f32_32x32x4_2b_bf16 v[32:63], v[126:127], v[128:129], 0\n" \
  "ds_read_b32 v126, v131 offset:14400\nv_exp_f32 v96, v64\nv_exp_f32 v104, v65\nv_exp_f32 v97, v66\nv_exp_f32 v105, v67\nv_cvt_pk_bf16_f32 v96, v96, v104\n" \
  "v_cvt_pk_bf16_f32 v97, v97, v105\ns_add_u32 m0, s41, 90112\ns_add_u32 s47, s40, 90112\nbuffer_load_dwordx4 v133, %[rkv], s47 offen lds\ns_add_u32 m0, s41, 98304\ns_add_u32 s47, s40, 98304\n" \
  "buffer_load_dwordx4 v133, %[rkv], s47 offen lds\nv_exp_f32 v98, v68\nv_exp_f32 v104, v69\nv_exp_f32 v99, v70\nv_exp_f32 v105, v71\nv_cvt_pk_bf16_f32 v98, v98, v104\n" \
  "v_cvt_pk_bf16_f32 v99, v99, v105\nv_mfma_f32_32x32x16_bf16 v[16:31], v[114:117], v[100:103], v[16:31]\nds_read_b128 v[114:117], v132 offset:5424\nv_exp_f32 v100, v72\nv_exp_f32 v104, v73\nv_exp_f32 v101, v74\n" \
  "v_exp_f32 v105, v75\nv_cvt_pk_bf16_f32 v100, v100, v104\nv_cvt_pk_bf16_f32 v101, v101, v105\ns_waitcnt lgkmcnt(2)\nv_mfma_f32_32x32x16_bf16 v[0:15], v[110:113], v[96:99], v[0:15]\nv_exp_f32 v102, v76\n" \
  "v_exp_f32 v104, v77\nv_exp_f32 v103, v78\nv_exp_f32 v105, v79\nv_cvt_pk_bf16_f32 v102, v102, v104\nv_cvt_pk_bf16_f32 v103, v103, v105\nv_mfma_f32_32x32x16_bf16 v[32:47], v[106:109], v[118:121], v[32:47]\n" \
  "s_add_u32 m0, s41, 106496\ns_add_u32 s47, s40, 106496\nbuffer_load_dwordx4 v133, %[rkv], s47 offen lds\ns_add_u32 m0, s41, 114688\ns_add_u32 s47, s40, 114688\nbuffer_load_dwordx4 v133, %[rkv], s47 offen lds\n" \
  "v_exp_f32 v96, v80\nv_exp_f32 v104, v81\nv_exp_f32 v97, v82\nv_exp_f32 v105, v83\nv_cvt_pk_bf16_f32 v96, v96, v104\nv_cvt_pk_bf16_f32 v97, v97, v105\n" \
  "s_waitcnt lgkmcnt(0)\nv_mfma_f32_32x32x16_bf16 v[0:15], v[114:117], v[100:103], v[0:15]\ns_add_u32 m0, s41, 122880\ns_add_u32 s47, s40, 122880\nbuffer_load_dwordx4 v133, %[rkv], s47 offen lds\ns_add_u32 m0, s41, 131072\n" \
  "s_add_u32 s47, s40, 131072\nbuffer_load_dwordx4 v133, %[rkv], s47 offen lds\nv_exp_f32 v98, v84\nv_exp_f32 v104, v85\nv_exp_f32 v99, v86\nv_exp_f32 v105, v87\n" \
  "v_cvt_pk_bf16_f32 v98, v98, v104\nv_cvt_pk_bf16_f32 v99, v99, v105\nv_mfma_f32_32x32x16_bf16 v[48:63], v[106:109], v[122:125], v[48:63]\nds_read_b128 v[106:109], v130 offset:14400\ns_setprio 1\nv_exp_f32 v100, v88\n" \
  "v_exp_f32 v104, v89\nv_exp_f32 v101, v90\nv_exp_f32 v105, v91\nv_cvt_pk_bf16_f32 v100, v100, v104\nv_cvt_pk_bf16_f32 v101, v101, v105\nv_mfma_f32_32x32x16_bf16 v[16:31], v[110:113], v[96:99], v[16:31]\n" \
  "ds_read_b128 v[110:113], v132 offset:9600\ns_add_u32 m0, s41, 139264\ns_add_u32 s47, s40, 139264\nbuffer_load_dwordx4 v133, %[rkv], s47 offen lds\ns_mov_b32 m0, s46\ns_nop 0\n" \
  "buffer_load_dwordx4 v133, %[rkv], s45 offen lds\nv_exp_f32 v102, v92\nv_exp_f32 v104, v93\nv_exp_f32 v103, v94\nv_exp_f32 v105, v95\nv_cvt_pk_bf16_f32 v102, v102, v104\n" \
  "v_cvt_pk_bf16_f32 v103, v103, v105\nv_mfma_f32_32x32x4_2b_bf16 v[64:95], v[126:127], v[128:129], 0\nds_read_b32 v126, v131 offset:19200\ns_waitcnt vmcnt(12)\ns_barrier\ns_setprio 3\n" \
  "v_exp_f32 v96, v32\nv_exp_f32 v104, v33\nv_exp_f32 v97, v34\nv_exp_f32 v105, v35\nv_cvt_pk_bf16_f32 v96, v96, v104\nv_cvt_pk_bf16_f32 v97, v97, v105\n" \
  "buffer_load_dword v208, v134, %[rsrc], s48 offen nt\ns_add_u32 s48, s48, s49\nbuffer_load_dword v209, v134, %[rsrc], s48 offen nt\ns_add_u32 s48, s48, s49\nv_exp_f32 v98, v36\nv_exp_f32 v104, v37\n" \
  "v_exp_f32 v99, v38\nv_exp_f32 v105, v39\nv_cvt_pk_bf16_f32 v98, v98, v104\nv_cvt_pk_bf16_f32 v99, v99, v105\nv_mfma_f32_32x32x16_bf16 v[16:31], v[114:117], v[100:103], v[16:31]\nds_read_b128 v[114:117], v132 offset:10224\n" \
  "v_exp_f32 v100, v40\nv_exp_f32 v104, v41\nv_exp_f32 v101, v42\nv_exp_f32 v105, v43\nv_cvt_pk_bf16_f32 v100, v100, v104\nv_cvt_pk_bf16_f32 v101, v101, v105\n" \
  "s_waitcnt lgkmcnt(2)\nv_mfma_f32_32x32x16_bf16 v[0:15], v[110:113], v[96:99], v[0:15]\nv_exp_f32 v102, v44\nv_exp_f32 v104, v45\nv_exp_f32 v103, v46\nv_exp_f32 v105, v47\n" \
  "v_cvt_pk_bf16_f32 v102, v102, v104\nv_cvt_pk_bf16_f32 v103, v103, v105\nv_mfma_f32_32x32x16_bf16 v[64:79], v[106:109], v[118:121], v[64:79]\nbuffer_load_dwordx4 v[136:139], v133, %[rwo], s42 offen offset:0\nv_exp_f32 v96, v48\nv_exp_f32 v104, v49\n" \
  "v_exp_f32 v97, v50\nv_exp_f32 v105, v51\nv_cvt_pk_bf16_f32 v96, v96, v104\nv_cvt_pk_bf16_f32 v97, v97, v105\ns_waitcnt lgkmcnt(0)\nv_mfma_f32_32x32x16_bf16 v[0:15], v[114:117], v[100:103], v[0:15]\n" \
  "buffer_load_dword v210, v134, %[rsrc], s48 offen nt\ns_add_u32 s48, s48, s49\nv_exp_f32 v98, v52\nv_exp_f32 v104, v53\nv_exp_f32 v99, v54\nv_exp_f32 v105, v55\n" \
  "v_cvt_pk_bf16_f32 v98, v98, v104\nv_cvt_pk_bf16_f32 v99, v99, v105\nv_mfma_f32_32x32x16_bf16 v[80:95], v[106:109], v[122:125], v[80:95]\nds_read_b128 v[106:109], v130 offset:19200\nv_exp_f32 v100, v56\nv_exp_f32 v104, v57\n" \
  "v_exp_f32 v101, v58\nv_exp_f32 v105, v59\nv_cvt_pk_bf16_f32 v100, v100, v104\nv_cvt_pk_bf16_f32 v101, v101, v105\nv_mfma_f32_32x32x16_bf16 v[16:31], v[110:113], v[96:99], v[16:31]\nds_read_b128 v[110:113], v132 offset:14400\n" \
  "buffer_load_dwordx4 v[140:143], v133, %[rwo], s42 offen offset:1024\nv_exp_f32 v102, v60\nv_exp_f32 v104, v61\nv_exp_f32 v103, v62\nv_exp_f32 v105, v63\nv_cvt_pk_bf16_f32 v102, v102, v104\n" \
  "v_cvt_pk_bf16_f32 v103, v103, v105\nv_mfma_f32_32x32x4_2b_bf16 v[32:63], v[126:127], v[128:129], 0\nds_read_b32 v126, v131 offset:24000\nv_exp_f32 v96, v64\nv_exp_f32 v104, v65\nv_exp_f32 v97, v66\n" \
  "v_exp_f32 v105, v67\nv_cvt_pk_bf16_f32 v96, v96, v104\nv_cvt_pk_bf16_f32 v97, v97, v105\nbuffer_load_dword v211, v134, %[rsrc], s48 offen nt\ns_add_u32 s48, s48, s49\nbuffer_load_dword v212, v134, %[rsrc], s48 offen nt\n" \
  "s_add_u32 s48, s48, s49\nv_exp_f32 v98, v68\nv_exp_f32 v104, v69\nv_exp_f32 v99, v70\nv_exp_f32 v105, v71\nv_cvt_pk_bf16_f32 v98, v98, v104\n" \
  "v_cvt_pk_bf16_f32 v99, v99, v105\nv_mfma_f32_32x32x16_bf16 v[16:31], v[114:117], v[100:103], v[16:31]\nds_read_b128 v[114:117], v132 offset:15024\nv_exp_f32 v100, v72\nv_exp_f32 v104, v73\nv_exp_f32 v101, v74\n" \
  "v_exp_f32 v105, v75\nv_cvt_pk_bf16_f32 v100, v100, v104\nv_cvt_pk_bf16_f32 v101, v101, v105\ns_waitcnt lgkmcnt(2)\nv_mfma_f32_32x32x16_bf16 v[0:15], v[110:113], v[96:99], v[0:15]\nv_exp_f32 v102, v76\n" \
  "v_exp_f32 v104, v77\nv_exp_f32 v103, v78\nv_exp_f32 v105, v79\nv_cvt_pk_bf16_f32 v102, v102, v104\nv_cvt_pk_bf16_f32 v103, v103, v105\nv_mfma_f32_32x32x16_bf16 v[32:47], v[106:109], v[118:121], v[32:47]\n" \
  "buffer_load_dwordx4 v[144:147], v133, %[rwo], s42 offen offset:2048\nv_exp_f32 v96, v80\nv_exp_f32 v104, v81\nv_exp_f32 v97, v82\nv_exp_f32 v105, v83\nv_cvt_pk_bf16_f32 v96, v96, v104\n" \
  "v_cvt_pk_bf16_f32 v97, v97, v105\ns_waitcnt lgkmcnt(0)\nv_mfma_f32_32x32x16_bf16 v[0:15], v[114:117], v[100:103], v[0:15]\nbuffer_load_dword v213, v134, %[rsrc], s48 offen nt\ns_add_u32 s48, s48, s49\nv_exp_f32 v98, v84\n" \
  "v_exp_f32 v104, v85\nv_exp_f32 v99, v86\nv_exp_f32 v105, v87\nv_cvt_pk_bf16_f32 v98, v98, v104\nv_cvt_pk_bf16_f32 v99, v99, v105\nv_mfma_f32_32x32x16_bf16 v[48:63], v[106:109], v[122:125], v[48:63]\n" \
  "ds_read_b128 v[106:109], v130 offset:24000\nv_exp_f32 v100, v88\nv_exp_f32 v104, v89\nv_exp_f32 v101, v90\nv_exp_f32 v105, v91\nv_cvt_pk_bf16_f32 v100, v100, v104\n" \
  "v_cvt_pk_bf16_f32 v101, v101, v105\nv_mfma_f32_32x32x16_bf16 v[16:31], v[110:113], v[96:99], v[16:31]\nds_read_b128 v[110:113], v132 offset:19200\nbuffer_load_dwordx4 v[148:151], v133, %[rwo], s42 offen offset:3072\nv_exp_f32 v102, v92\nv_exp_f32 v104, v93\n" \
  "v_exp_f32 v103, v94\nv_exp_f32 v105, v95\nv_cvt_pk_bf16_f32 v102, v102, v104\nv_cvt_pk_bf16_f32 v103, v103, v105\nv_mfma_f32_32x32x4_2b_bf16 v[64:95], v[126:127], v[128:129], 0\nds_read_b32 v126, v131 offset:28800\n" \
  "s_setprio 2\nv_exp_f32 v96, v32\nv_exp_f32 v104, v33\nv_exp_f32 v97, v34\nv_exp_f32 v105, v35\nv_cvt_pk_bf16_f32 v96, v96, v104\n" \
  "v_cvt_pk_bf16_f32 v97, v97, v105\nbuffer_load_dword v214, v134, %[rsrc], s48 offen nt\ns_add_u32 s48, s48, s49\nv_exp_f32 v98, v36\nv_exp_f32 v104, v37\nv_exp_f32 v99, v38\n" \
  "v_exp_f32 v105, v39\nv_cvt_pk_bf16_f32 v98, v98, v104\nv_cvt_pk_bf16_f32 v99, v99, v105\nv_mfma_f32_32x32x16_bf16 v[16:31], v[114:117], v[100:103], v[16:31]\nds_read_b128 v[114:117], v132 offset:19824\nv_exp_f32 v100, v40\n" \
  "v_exp_f32 v104, v41\nv_exp_f32 v101, v42\nv_exp_f32 v105, v43\nv_cvt_pk_bf16_f32 v100, v100, v104\nv_cvt_pk_bf16_f32 v101, v101, v105\ns_waitcnt lgkmcnt(2)\n" \
  "v_mfma_f32_32x32x16_bf16 v[0:15], v[110:113], v[96:99], v[0:15]\nv_exp_f32 v102, v44\nv_exp_f32 v104, v45\nv_exp_f32 v103, v46\nv_exp_f32 v105, v47\nv_cvt_pk_bf16_f32 v102, v102, v104\n" \
  "v_cvt_pk_bf16_f32 v103, v103, v105\nv_mfma_f32_32x32x16_bf16 v[64:79], v[106:109], v[118:121], v[64:79]\ns_add_u32 s47, s42, 4096\nbuffer_load_dwordx4 v[152:155], v133, %[rwo], s47 offen offset:0\nv_exp_f32 v96, v48\nv_exp_f32 v104, v49\n" \
  "v_exp_f32 v97, v50\nv_exp_f32 v105, v51\nv_cvt_pk_bf16_f32 v96, v96, v104\nv_cvt_pk_bf16_f32 v97, v97, v105\ns_waitcnt lgkmcnt(0)\nv_mfma_f32_32x32x16_bf16 v[0:15], v[114:117], v[100:103], v[0:15]\n" \
  "buffer_load_dword v215, v134, %[rsrc], s48 offen nt\ns_add_u32 s48, s48, s49\nbuffer_load_dword v216, v134, %[rsrc], s48 offen nt\ns_add_u32 s48, s48, s49\nv_exp_f32 v98, v52\nv_exp_f32 v104, v53\n" \
  "v_exp_f32 v99, v54\nv_exp_f32 v105, v55\nv_cvt_pk_bf16_f32 v98, v98, v104\nv_cvt_pk_bf16_f32 v99, v99, v105\nv_mfma_f32_32x32x16_bf16 v[80:95], v[106:109], v[122:125], v[80:95]\nds_read_b128 v[106:109], v130 offset:28800\n" \
  "v_exp_f32 v100, v56\nv_exp_f32 v104, v57\nv_exp_f32 v101, v58\nv_exp_f32 v105, v59\nv_cvt_pk_bf16_f32 v100, v100, v104\nv_cvt_pk_bf16_f32 v101, v101, v105\n" \
  "v_mfma_f32_32x32x16_bf16 v[16:31], v[110:113], v[96:99], v[16:31]\nds_read_b128 v[110:113], v132 offset:24000\ns_add_u32 s47, s42, 4096\nbuffer_load_dwordx4 v[156:159], v133, %[rwo], s47 offen offset:1024\nv_exp_f32 v102, v60\nv_exp_f32 v104, v61\n" \
  "v_exp_f32 v103, v62\nv_exp_f32 v105, v63\nv_cvt_pk_bf16_f32 v102, v102, v104\nv_cvt_pk_bf16_f32 v103, v103, v105\nv_mfma_f32_32x32x4_2b_bf16 v[32:63], v[126:127], v[128:129], 0\nds_read_b32 v126, v131 offset:33600\n" \
  "v_exp_f32 v96, v64\nv_exp_f32 v104, v65\nv_exp_f32 v97, v66\nv_exp_f32 v105, v67\nv_cvt_pk_bf16_f32 v96, v96, v104\nv_cvt_pk_bf16_f32 v97, v97, v105\n" \
  "buffer_load_dword v217, v134, %[rsrc], s48 offen nt\ns_add_u32 s48, s48, s49\nv_exp_f32 v98, v68\nv_exp_f32 v104, v69\nv_exp_f32 v99, v70\nv_exp_f32 v105, v71\n" \
  "v_cvt_pk_bf16_f32 v98, v98, v104\nv_cvt_pk_bf16_f32 v99, v99, v105\nv_mfma_f32_32x32x16_bf16 v[16:31], v[114:117], v[100:103], v[16:31]\nds_read_b128 v[114:117], v132 offset:24624\nv_exp_f32 v100, v72\nv_exp_f32 v104, v73\n" \
  "v_exp_f32 v101, v74\nv_exp_f32 v105, v75\nv_cvt_pk_bf16_f32 v100, v100, v104\nv_cvt_pk_bf16_f32 v101, v101, v105\ns_waitcnt lgkmcnt(2)\nv_mfma_f32_32x32x16_bf16 v[0:15], v[110:113], v[96:99], v[0:15]\n" \
  "v_exp_f32 v102, v76\nv_exp_f32 v104, v77\nv_exp_f32 v103, v78\nv_exp_f32 v105, v79\nv_cvt_pk_bf16_f32 v102, v102, v104\nv_cvt_pk_bf16_f32 v103, v103, v105\n" \
  "v_mfma_f32_32x32x16_bf16 v[32:47], v[106:109], v[118:121], v[32:47]\ns_add_u32 s47, s42, 4096\nbuffer_load_dwordx4 v[160:163], v133, %[rwo], s47 offen offset:2048\nv_exp_f32 v96, v80\nv_exp_f32 v104, v81\nv_exp_f32 v97, v82\n" \
  "v_exp_f32 v105, v83\nv_cvt_pk_bf16_f32 v96, v96, v104\nv_cvt_pk_bf16_f32 v97, v97, v105\ns_waitcnt lgkmcnt(0)\nv_mfma_f32_32x32x16_bf16 v[0:15], v[114:117], v[100:103], v[0:15]\nbuffer_load_dword v218, v134, %[rsrc], s48 offen nt\n" \
  "s_add_u32 s48, s48, s49\nv_exp_f32 v98, v84\nv_exp_f32 v104, v85\nv_exp_f32 v99, v86\nv_exp_f32 v105, v87\nv_cvt_pk_bf16_f32 v98, v98, v104\n" \
  "v_cvt_pk_bf16_f32 v99, v99, v105\nv_mfma_f32_32x32x16_bf16 v[48:63], v[106:109], v[122:125], v[48:63]\nds_read_b128 v[106:109], v130 offset:33600\nv_exp_f32 v100, v88\nv_exp_f32 v104, v89\nv_exp_f32 v101, v90\n" \
  "v_exp_f32 v105, v91\nv_cvt_pk_bf16_f32 v100, v100, v104\nv_cvt_pk_bf16_f32 v101, v101, v105\nv_mfma_f32_32x32x16_bf16 v[16:31], v[110:113], v[96:99], v[16:31]\nds_read_b128 v[110:113], v132 offset:28800\ns_add_u32 s47, s42, 4096\n" \
  "buffer_load_dwordx4 v[164:167], v133, %[rwo], s47 offen offset:3072\nv_exp_f32 v102, v92\nv_exp_f32 v104, v93\nv_exp_f32 v103, v94\nv_exp_f32 v105, v95\nv_cvt_pk_bf16_f32 v102, v102, v104\n" \
  "v_cvt_pk_bf16_f32 v103, v103, v105\nv_mfma_f32_32x32x4_2b_bf16 v[64:95], v[126:127], v[128:129], 0\nds_read_b32 v126, v131 offset:38400\ns_setprio 1\nv_exp_f32 v96, v32\nv_exp_f32 v104, v33\n" \
  "v_exp_f32 v97, v34\nv_exp_f32 v105, v35\nv_cvt_pk_bf16_f32 v96, v96, v104\nv_cvt_pk_bf16_f32 v97, v97, v105\nbuffer_load_dword v219, v134, %[rsrc], s48 offen nt\ns_add_u32 s48, s48, s49\n" \
  "buffer_load_dword v220, v134, %[rsrc], s48 offen nt\ns_add_u32 s48, s48, s49\nv_exp_f32 v98, v36\nv_exp_f32 v104, v37\nv_exp_f32 v99, v38\nv_exp_f32 v105, v39\n" \
  "v_cvt_pk_bf16_f32 v98, v98, v104\nv_cvt_pk_bf16_f32 v99, v99, v105\nv_mfma_f32_32x32x16_bf16 v[16:31], v[114:117], v[100:103], v[16:31]\nds_read_b128 v[114:117], v132 offset:29424\nv_exp_f32 v100, v40\nv_exp_f32 v104, v41\n" \
  "v_exp_f32 v101, v42\nv_exp_f32 v105, v43\nv_cvt_pk_bf16_f32 v100, v100, v104\nv_cvt_pk_bf16_f32 v101, v101, v105\ns_waitcnt lgkmcnt(2)\nv_mfma_f32_32x32x16_bf16 v[0:15], v[110:113], v[96:99], v[0:15]\n" \
  "v_exp_f32 v102, v44\nv_exp_f32 v104, v45\nv_exp_f32 v103, v46\nv_exp_f32 v105, v47\nv_cvt_pk_bf16_f32 v102, v102, v104\nv_cvt_pk_bf16_f32 v103, v103, v105\n" \
  "v_mfma_f32_32x32x16_bf16 v[64:79], v[106:109], v[118:121], v[64:79]\ns_add_u32 s47, s42, 8192\nbuffer_load_dwordx4 v[168:171], v133, %[rwo], s47 offen offset:0\nv_exp_f32 v96, v48\nv_exp_f32 v104, v49\nv_exp_f32 v97, v50\n" \
  "v_exp_f32 v105, v51\nv_cvt_pk_bf16_f32 v96, v96, v104\nv_cvt_pk_bf16_f32 v97, v97, v105\ns_waitcnt lgkmcnt(0)\nv_mfma_f32_32x32x16_bf16 v[0:15], v[114:117], v[100:103], v[0:15]\nbuffer_load_dword v221, v134, %[rsrc], s48 offen nt\n" \
  "s_add_u32 s48, s48, s49\nv_exp_f32 v98, v52\nv_exp_f32 v104, v53\nv_exp_f32 v99, v54\nv_exp_f32 v105, v55\nv_cvt_pk_bf16_f32 v98, v98, v104\n" \
  "v_cvt_pk_bf16_f32 v99, v99, v105\nv_mfma_f32_32x32x16_bf16 v[80:95], v[106:109], v[122:125], v[80:95]\nds_read_b128 v[106:109], v130 offset:38400\ns_setprio 0\nv_exp_f32 v100, v56\nv_exp_f32 v104, v57\n" \
  "v_exp_f32 v101, v58\nv_exp_f32 v105, v59\nv_cvt_pk_bf16_f32 v100, v100, v104\nv_cvt_pk_bf16_f32 v101, v101, v105\nv_mfma_f32_32x32x16_bf16 v[16:31], v[110:113], v[96:99], v[16:31]\nds_read_b128 v[110:113], v132 offset:33600\n" \
  "buffer_load_dwordx4 v[172:175], v133, %[rwo], s43 offen offset:0\nv_exp_f32 v102, v60\nv_exp_f32 v104, v61\nv_exp_f32 v103, v62\nv_exp_f32 v105, v63\nv_cvt_pk_bf16_f32 v102, v102, v104\n" \
  "v_cvt_pk_bf16_f32 v103, v103, v105\nv_mfma_f32_32x32x4_2b_bf16 v[32:63], v[126:127], v[128:129], 0\nds_read_b32 v126, v131 offset:43200\ns_waitcnt vmcnt(24)\ns_barrier\ns_setprio 3\n" \
  "v_exp_f32 v96, v64\nv_exp_f32 v104, v65\nv_exp_f32 v97, v66\nv_exp_f32 v105, v67\nv_cvt_pk_bf16_f32 v96, v96, v104\nv_cvt_pk_bf16_f32 v97, v97, v105\n" \
  "buffer_load_dword v222, v134, %[rsrc], s48 offen nt\ns_add_u32 s48, s48, s49\nv_exp_f32 v98, v68\nv_exp_f32 v104, v69\nv_exp_f32 v99, v70\nv_exp_f32 v105, v71\n" \
  "v_cvt_pk_bf16_f32 v98, v98, v104\nv_cvt_pk_bf16_f32 v99, v99, v105\nv_mfma_f32_32x32x16_bf16 v[16:31], v[114:117], v[100:103], v[16:31]\nds_read_b128 v[114:117], v132 offset:34224\nv_exp_f32 v100, v72\nv_exp_f32 v104, v73\n" \
  "v_exp_f32 v101, v74\nv_exp_f32 v105, v75\nv_cvt_pk_bf16_f32 v100, v100, v104\nv_cvt_pk_bf16_f32 v101, v101, v105\ns_waitcnt lgkmcnt(2)\nv_mfma_f32_32x32x16_bf16 v[0:15], v[110:113], v[96:99], v[0:15]\n" \
  "v_exp_f32 v102, v76\nv_exp_f32 v104, v77\nv_exp_f32 v103, v78\nv_exp_f32 v105, v79\nv_cvt_pk_bf16_f32 v102, v102, v104\nv_cvt_pk_bf16_f32 v103, v103, v105\n" \
  "v_mfma_f32_32x32x16_bf16 v[32:47], v[106:109], v[118:121], v[32:47]\nbuffer_load_dwordx4 v[176:179], v133, %[rwo], s43 offen offset:1024\nv_exp_f32 v96, v80\nv_exp_f32 v104, v81\nv_exp_f32 v97, v82\nv_exp_f32 v105, v83\n" \
  "v_cvt_pk_bf16_f32 v96, v96, v104\nv_cvt_pk_bf16_f32 v97, v97, v105\ns_waitcnt lgkmcnt(0)\nv_mfma_f32_32x32x16_bf16 v[0:15], v[114:117], v[100:103], v[0:15]\nbuffer_load_dword v223, v134, %[rsrc], s48 offen nt\ns_add_u32 s48, s48, s49\n" \
  "s_add_u32 s48, s48, s50\nbuffer_load_dword v224, v134, %[rsrc], s48 offen nt\ns_add_u32 s48, s48, s49\nv_exp_f32 v98, v84\nv_exp_f32 v104, v85\nv_exp_f32 v99, v86\n" \
  "v_exp_f32 v105, v87\nv_cvt_pk_bf16_f32 v98, v98, v104\nv_cvt_pk_bf16_f32 v99, v99, v105\nv_mfma_f32_32x32x16_bf16 v[48:63], v[106:109], v[122:125], v[48:63]\nds_read_b128 v[106:109], v130 offset:43200\nv_exp_f32 v100, v88\n" \
  "v_exp_f32 v104, v89\nv_exp_f32 v101, v90\nv_exp_f32 v105, v91\nv_cvt_pk_bf16_f32 v100, v100, v104\nv_cvt_pk_bf16_f32 v101, v101, v105\nv_mfma_f32_32x32x16_bf16 v[16:31], v[110:113], v[96:99], v[16:31]\n" \
  "ds_read_b128 v[110:113], v132 offset:38400\nbuffer_load_dwordx4 v[180:183], v133, %[rwo], s43 offen offset:2048\nv_exp_f32 v102, v92\nv_exp_f32 v104, v93\nv_exp_f32 v103, v94\nv_exp_f32 v105, v95\n" \
  "v_cvt_pk_bf16_f32 v102, v102, v104\nv_cvt_pk_bf16_f32 v103, v103, v105\nv_mfma_f32_32x32x4_2b_bf16 v[64:95], v[126:127], v[128:129], 0\nds_read_b32 v126, v131 offset:48000\nv_add_u32 v130, 38400, v130\nv_add_u32 v131, 38400, v131\n" \
  "v_add_u32 v132, 38400, v132\nv_exp_f32 v96, v32\nv_exp_f32 v104, v33\nv_exp_f32 v97, v34\nv_exp_f32 v105, v35\nv_cvt_pk_bf16_f32 v96, v96, v104\n" \
  "v_cvt_pk_bf16_f32 v97, v97, v105\nbuffer_load_dword v225, v134, %[rsrc], s48 offen nt\ns_add_u32 s48, s48, s49\nv_exp_f32 v98, v36\nv_exp_f32 v104, v37\nv_exp_f32 v99, v38\n" \
  "v_exp_f32 v105, v39\nv_cvt_pk_bf16_f32 v98, v98, v104\nv_cvt_pk_bf16_f32 v99, v99, v105\nv_mfma_f32_32x32x16_bf16 v[16:31], v[114:117], v[100:103], v[16:31]\nds_read_b128 v[114:117], v132 offset:624\nv_exp_f32 v100, v40\n" \
  "v_exp_f32 v104, v41\nv_exp_f32 v101, v42\nv_exp_f32 v105, v43\nv_cvt_pk_bf16_f32 v100, v100, v104\nv_cvt_pk_bf16_f32 v101, v101, v105\ns_waitcnt lgkmcnt(2)\n" \
  "v_mfma_f32_32x32x16_bf16 v[0:15], v[110:113], v[96:99], v[0:15]\nv_exp_f32 v102, v44\nv_exp_f32 v104, v45\nv_exp_f32 v103, v46\nv_exp_f32 v105, v47\nv_cvt_pk_bf16_f32 v102, v102, v104\n" \
  "v_cvt_pk_bf16_f32 v103, v103, v105\nv_mfma_f32_32x32x16_bf16 v[64:79], v[106:109], v[118:121], v[64:79]\ns_waitcnt vmcnt(17)\nv_perm_b32 v240, v209, v208, s51\nv_perm_b32 v241, v211, v210, s51\nv_perm_b32 v242, v213, v212, s51\n" \
  "v_perm_b32 v243, v215, v214, s51\nbuffer_store_dwordx4 v[240:243], v135, %[rdst], 0 offen offset:0 sc1\nv_exp_f32 v96, v48\nv_exp_f32 v104, v49\nv_exp_f32 v97, v50\nv_exp_f32 v105, v51\n" \
  "v_cvt_pk_bf16_f32 v96, v96, v104\nv_cvt_pk_bf16_f32 v97, v97, v105\ns_waitcnt lgkmcnt(0)\nv_mfma_f32_32x32x16_bf16 v[0:15], v[114:117], v[100:103], v[0:15]\nbuffer_load_dword v226, v134, %[rsrc], s48 offen nt\ns_add_u32 s48, s48, s49\n" \
  "v_exp_f32 v98, v52\nv_exp_f32 v104, v53\nv_exp_f32 v99, v54\nv_exp_f32 v105, v55\nv_cvt_pk_bf16_f32 v98, v98, v104\nv_cvt_pk_bf16_f32 v99, v99, v105\n" \
  "v_mfma_f32_32x32x16_bf16 v[80:95], v[106:109], v[122:125], v[80:95]\nds_read_b128 v[106:109], v130 offset:9600\nv_exp_f32 v100, v56\nv_exp_f32 v104, v57\nv_exp_f32 v101, v58\nv_exp_f32 v105, v59\n" \
  "v_cvt_pk_bf16_f32 v100, v100, v104\nv_cvt_pk_bf16_f32 v101, v101, v105\nv_mfma_f32_32x32x16_bf16 v[16:31], v[110:113], v[96:99], v[16:31]\nds_read_b128 v[110:113], v132 offset:4800\nv_perm_b32 v240, v209, v208, s52\nv_perm_b32 v241, v211, v210, s52\n" \
  "v_perm_b32 v242, v213, v212, s52\nv_perm_b32 v243, v215, v214, s52\nbuffer_store_dwordx4 v[240:243], v244, %[rdst], 0 offen offset:0 sc1\nv_exp_f32 v102, v60\nv_exp_f32 v104, v61\nv_exp_f32 v103, v62\n" \
  "v_exp_f32 v105, v63\nv_cvt_pk_bf16_f32 v102, v102, v104\nv_cvt_pk_bf16_f32 v103, v103, v105\nv_mfma_f32_32x32x4_2b_bf16 v[32:63], v[126:127], v[128:129], 0\nds_read_b32 v126, v131 offset:14400\nv_exp_f32 v96, v64\n" \
  "v_exp_f32 v104, v65\nv_exp_f32 v97, v66\nv_exp_f32 v105, v67\nv_cvt_pk_bf16_f32 v96, v96, v104\nv_cvt_pk_bf16_f32 v97, v97, v105\nbuffer_load_dword v227, v134, %[rsrc], s48 offen nt\n" \
  "s_add_u32 s48, s48, s49\nbuffer_load_dword v228, v134, %[rsrc], s48 offen nt\ns_add_u32 s48, s48, s49\nv_exp_f32 v98, v68\nv_exp_f32 v104, v69\nv_exp_f32 v99, v70\n" \
  "v_exp_f32 v105, v71\nv_cvt_pk_bf16_f32 v98, v98, v104\nv_cvt_pk_bf16_f32 v99, v99, v105\nv_mfma_f32_32x32x16_bf16 v[16:31], v[114:117], v[100:103], v[16:31]\nds_read_b128 v[114:117], v132 offset:5424\nv_exp_f32 v100, v72\n" \
  "v_exp_f32 v104, v73\nv_exp_f32 v101, v74\nv_exp_f32 v105, v75\nv_cvt_pk_bf16_f32 v100, v100, v104\nv_cvt_pk_bf16_f32 v101, v101, v105\ns_waitcnt lgkmcnt(2)\n" \
  "v_mfma_f32_32x32x16_bf16 v[0:15], v[110:113], v[96:99], v[0:15]\nv_exp_f32 v102, v76\nv_exp_f32 v104, v77\nv_exp_f32 v103, v78\nv_exp_f32 v105, v79\nv_cvt_pk_bf16_f32 v102, v102, v104\n" \
  "v_cvt_pk_bf16_f32 v103, v103, v105\nv_mfma_f32_32x32x16_bf16 v[32:47], v[106:109], v[118:121], v[32:47]\nbuffer_load_dwordx4 v[184:187], v133, %[rwo], s43 offen offset:3072\nv_exp_f32 v96, v80\nv_exp_f32 v104, v81\nv_exp_f32 v97, v82\n" \
  "v_exp_f32 v105, v83\nv_cvt_pk_bf16_f32 v96, v96, v104\nv_cvt_pk_bf16_f32 v97, v97, v105\ns_waitcnt lgkmcnt(0)\nv_mfma_f32_32x32x16_bf16 v[0:15], v[114:117], v[100:103], v[0:15]\nbuffer_load_dword v229, v134, %[rsrc], s48 offen nt\n" \
  "s_add_u32 s48, s48, s49\nv_exp_f32 v98, v84\nv_exp_f32 v104, v85\nv_exp_f32 v99, v86\nv_exp_f32 v105, v87\nv_cvt_pk_bf16_f32 v98, v98, v104\n" \
  "v_cvt_pk_bf16_f32 v99, v99, v105\nv_mfma_f32_32x32x16_bf16 v[48:63], v[106:109], v[122:125], v[48:63]\nds_read_b128 v[106:109], v130 offset:14400\nv_exp_f32 v100, v88\nv_exp_f32 v104, v89\nv_exp_f32 v101, v90\n" \
  "v_exp_f32 v105, v91\nv_cvt_pk_bf16_f32 v100, v100, v104\nv_cvt_pk_bf16_f32 v101, v101, v105\nv_mfma_f32_32x32x16_bf16 v[16:31], v[110:113], v[96:99], v[16:31]\nds_read_b128 v[110:113], v132 offset:9600\ns_add_u32 s47, s43, 4096\n" \
  "buffer_load_dwordx4 v[188:191], v133, %[rwo], s47 offen offset:0\nv_exp_f32 v102, v92\nv_exp_f32 v104, v93\nv_exp_f32 v103, v94\nv_exp_f32 v105, v95\nv_cvt_pk_bf16_f32 v102, v102, v104\n" \
  "v_cvt_pk_bf16_f32 v103, v103, v105\nv_mfma_f32_32x32x4_2b_bf16 v[64:95], v[126:127], v[128:129], 0\nds_read_b32 v126, v131 offset:19200\ns_setprio 2\nv_exp_f32 v96, v32\nv_exp_f32 v104, v33\n" \
  "v_exp_f32 v97, v34\nv_exp_f32 v105, v35\nv_cvt_pk_bf16_f32 v96, v96, v104\nv_cvt_pk_bf16_f32 v97, v97, v105\nbuffer_load_dword v230, v134, %[rsrc], s48 offen nt\ns_add_u32 s48, s48, s49\n" \
  "v_exp_f32 v98, v36\nv_exp_f32 v104, v37\nv_exp_f32 v99, v38\nv_exp_f32 v105, v39\nv_cvt_pk_bf16_f32 v98, v98, v104\nv_cvt_pk_bf16_f32 v99, v99, v105\n" \
  "v_mfma_f32_32x32x16_bf16 v[16:31], v[114:117], v[100:103], v[16:31]\nds_read_b128 v[114:117], v132 offset:10224\nv_exp_f32 v100, v40\nv_exp_f32 v104, v41\nv_exp_f32 v101, v42\nv_exp_f32 v105, v43\n" \
  "v_cvt_pk_bf16_f32 v100, v100, v104\nv_cvt_pk_bf16_f32 v101, v101, v105\ns_waitcnt lgkmcnt(2)\nv_mfma_f32_32x32x16_bf16 v[0:15], v[110:113], v[96:99], v[0:15]\nv_exp_f32 v102, v44\nv_exp_f32 v104, v45\n" \
  "v_exp_f32 v103, v46\nv_exp_f32 v105, v47\nv_cvt_pk_bf16_f32 v102, v102, v104\nv_cvt_pk_bf16_f32 v103, v103, v105\nv_mfma_f32_32x32x16_bf16 v[64:79], v[106:109], v[118:121], v[64:79]\ns_add_u32 s47, s43, 4096\n" \
  "buffer_load_dwordx4 v[192:195], v133, %[rwo], s47 offen offset:1024\nv_exp_f32 v96, v48\nv_exp_f32 v104, v49\nv_exp_f32 v97, v50\nv_exp_f32 v105, v51\nv_cvt_pk_bf16_f32 v96, v96, v104\n" \
  "v_cvt_pk_bf16_f32 v97, v97, v105\ns_waitcnt lgkmcnt(0)\nv_mfma_f32_32x32x16_bf16 v[0:15], v[114:117], v[100:103], v[0:15]\nbuffer_load_dword v231, v134, %[rsrc], s48 offen nt\ns_add_u32 s48, s48, s49\nbuffer_load_dword v232, v134, %[rsrc], s48 offen nt\n" \
  "s_add_u32 s48, s48, s49\nv_exp_f32 v98, v52\nv_exp_f32 v104, v53\nv_exp_f32 v99, v54\nv_exp_f32 v105, v55\nv_cvt_pk_bf16_f32 v98, v98, v104\n" \
  "v_cvt_pk_bf16_f32 v99, v99, v105\nv_mfma_f32_32x32x16_bf16 v[80:95], v[106:109], v[122:125], v[80:95]\nds_read_b128 v[106:109], v130 offset:19200\nv_exp_f32 v100, v56\nv_exp_f32 v104, v57\nv_exp_f32 v101, v58\n" \
  "v_exp_f32 v105, v59\nv_cvt_pk_bf16_f32 v100, v100, v104\nv_cvt_pk_bf16_f32 v101, v101, v105\nv_mfma_f32_32x32x16_bf16 v[16:31], v[110:113], v[96:99], v[16:31]\nds_read_b128 v[110:113], v132 offset:14400\ns_add_u32 s47, s43, 4096\n" \
  "buffer_load_dwordx4 v[196:199], v133, %[rwo], s47 offen offset:2048\nv_exp_f32 v102, v60\nv_exp_f32 v104, v61\nv_exp_f32 v103, v62\nv_exp_f32 v105, v63\nv_cvt_pk_bf16_f32 v102, v102, v104\n" \
  "v_cvt_pk_bf16_f32 v103, v103, v105\nv_mfma_f32_32x32x4_2b_bf16 v[32:63], v[126:127], v[128:129], 0\nds_read_b32 v126, v131 offset:24000\nv_exp_f32 v96, v64\nv_exp_f32 v104, v65\nv_exp_f32 v97, v66\n" \
  "v_exp_f32 v105, v67\nv_cvt_pk_bf16_f32 v96, v96, v104\nv_cvt_pk_bf16_f32 v97, v97, v105\nbuffer_load_dword v233, v134, %[rsrc], s48 offen nt\ns_add_u32 s48, s48, s49\nv_exp_f32 v98, v68\n" \
  "v_exp_f32 v104, v69\nv_exp_f32 v99, v70\nv_exp_f32 v105, v71\nv_cvt_pk_bf16_f32 v98, v98, v104\nv_cvt_pk_bf16_f32 v99, v99, v105\nv_mfma_f32_32x32x16_bf16 v[16:31], v[114:117], v[100:103], v[16:31]\n" \
  "ds_read_b128 v[114:117], v132 offset:15024\nv_exp_f32 v100, v72\nv_exp_f32 v104, v73\nv_exp_f32 v101, v74\nv_exp_f32 v105, v75\nv_cvt_pk_bf16_f32 v100, v100, v104\n" \
  "v_cvt_pk_bf16_f32 v101, v101, v105\ns_waitcnt lgkmcnt(2)\nv_mfma_f32_32x32x16_bf16 v[0:15], v[110:113], v[96:99], v[0:15]\nv_exp_f32 v102, v76\nv_exp_f32 v104, v77\nv_exp_f32 v103, v78\n" \
  "v_exp_f32 v105, v79\nv_cvt_pk_bf16_f32 v102, v102, v104\nv_cvt_pk_bf16_f32 v103, v103, v105\nv_mfma_f32_32x32x16_bf16 v[32:47], v[106:109], v[118:121], v[32:47]\ns_add_u32 s47, s43, 4096\nbuffer_load_dwordx4 v[200:203], v133, %[rwo], s47 offen offset:3072\n" \
  "v_exp_f32 v96, v80\nv_exp_f32 v104, v81\nv_exp_f32 v97, v82\nv_exp_f32 v105, v83\nv_cvt_pk_bf16_f32 v96, v96, v104\nv_cvt_pk_bf16_f32 v97, v97, v105\n" \
  "s_waitcnt lgkmcnt(0)\nv_mfma_f32_32x32x16_bf16 v[0:15], v[114:117], v[100:103], v[0:15]\nbuffer_load_dword v234, v134, %[rsrc], s48 offen nt\ns_add_u32 s48, s48, s49\nbuffer_load_dword v235, v134, %[rsrc], s48 offen nt\ns_add_u32 s48, s48, s49\n" \
  "v_exp_f32 v98, v84\nv_exp_f32 v104, v85\nv_exp_f32 v99, v86\nv_exp_f32 v105, v87\nv_cvt_pk_bf16_f32 v98, v98, v104\nv_cvt_pk_bf16_f32 v99, v99, v105\n" \
  "v_mfma_f32_32x32x16_bf16 v[48:63], v[106:109], v[122:125], v[48:63]\nds_read_b128 v[106:109], v130 offset:24000\nv_exp_f32 v100, v88\nv_exp_f32 v104, v89\nv_exp_f32 v101, v90\nv_exp_f32 v105, v91\n" \
  "v_cvt_pk_bf16_f32 v100, v100, v104\nv_cvt_pk_bf16_f32 v101, v101, v105\nv_mfma_f32_32x32x16_bf16 v[16:31], v[110:113], v[96:99], v[16:31]\nds_read_b128 v[110:113], v132 offset:19200\ns_add_u32 s47, s43, 8192\nbuffer_load_dwordx4 v[204:207], v133, %[rwo], s47 offen offset:0\n" \
  "v_exp_f32 v102, v92\nv_exp_f32 v104, v93\nv_exp_f32 v103, v94\nv_exp_f32 v105, v95\nv_cvt_pk_bf16_f32 v102, v102, v104\nv_cvt_pk_bf16_f32 v103, v103, v105\n" \
  "v_mfma_f32_32x32x4_2b_bf16 v[64:95], v[126:127], v[128:129], 0\nds_read_b32 v126, v131 offset:28800\nv_exp_f32 v96, v32\nv_exp_f32 v104, v33\nv_exp_f32 v97, v34\nv_exp_f32 v105, v35\n" \
  "v_cvt_pk_bf16_f32 v96, v96, v104\nv_cvt_pk_bf16_f32 v97, v97, v105\nbuffer_load_dword v236, v134, %[rsrc], s48 offen nt\ns_add_u32 s48, s48, s49\nv_exp_f32 v98, v36\nv_exp_f32 v104, v37\n" \
  "v_exp_f32 v99, v38\nv_exp_f32 v105, v39\nv_cvt_pk_bf16_f32 v98, v98, v104\nv_cvt_pk_bf16_f32 v99, v99, v105\nv_mfma_f32_32x32x16_bf16 v[16:31], v[114:117], v[100:103], v[16:31]\nds_read_b128 v[114:117], v132 offset:19824\n" \
  "v_exp_f32 v100, v40\nv_exp_f32 v104, v41\nv_exp_f32 v101, v42\nv_exp_f32 v105, v43\nv_cvt_pk_bf16_f32 v100, v100, v104\nv_cvt_pk_bf16_f32 v101, v101, v105\n" \
  "s_waitcnt lgkmcnt(2)\nv_mfma_f32_32x32x16_bf16 v[0:15], v[110:113], v[96:99], v[0:15]\nv_exp_f32 v102, v44\nv_exp_f32 v104, v45\nv_exp_f32 v103, v46\nv_exp_f32 v105, v47\n" \
  "v_cvt_pk_bf16_f32 v102, v102, v104\nv_cvt_pk_bf16_f32 v103, v103, v105\nv_mfma_f32_32x32x16_bf16 v[64:79], v[106:109], v[118:121], v[64:79]\ns_waitcnt vmcnt(22)\nv_perm_b32 v240, v217, v216, s51\nv_perm_b32 v241, v219, v218, s51\n" \
  "v_perm_b32 v242, v221, v220, s51\nv_perm_b32 v243, v223, v222, s51\nbuffer_store_dwordx4 v[240:243], v135, %[rdst], 0 offen offset:256 sc1\nv_exp_f32 v96, v48\nv_exp_f32 v104, v49\nv_exp_f32 v97, v50\n" \
  "v_exp_f32 v105, v51\nv_cvt_pk_bf16_f32 v96, v96, v104\nv_cvt_pk_bf16_f32 v97, v97, v105\ns_waitcnt lgkmcnt(0)\nv_mfma_f32_32x32x16_bf16 v[0:15], v[114:117], v[100:103], v[0:15]\nbuffer_load_dword v237, v134, %[rsrc], s48 offen nt\n" \
  "s_add_u32 s48, s48, s49\nv_exp_f32 v98, v52\nv_exp_f32 v104, v53\nv_exp_f32 v99, v54\nv_exp_f32 v105, v55\nv_cvt_pk_bf16_f32 v98, v98, v104\n" \
  "v_cvt_pk_bf16_f32 v99, v99, v105\nv_mfma_f32_32x32x16_bf16 v[80:95], v[106:109], v[122:125], v[80:95]\nds_read_b128 v[106:109], v130 offset:28800\nv_exp_f32 v100, v56\nv_exp_f32 v104, v57\nv_exp_f32 v101, v58\n" \
  "v_exp_f32 v105, v59\nv_cvt_pk_bf16_f32 v100, v100, v104\nv_cvt_pk_bf16_f32 v101, v101, v105\nv_mfma_f32_32x32x16_bf16 v[16:31], v[110:113], v[96:99], v[16:31]\nds_read_b128 v[110:113], v132 offset:24000\nv_perm_b32 v240, v217, v216, s52\n" \
  "v_perm_b32 v241, v219, v218, s52\nv_perm_b32 v242, v221, v220, s52\nv_perm_b32 v243, v223, v222, s52\nbuffer_store_dwordx4 v[240:243], v244, %[rdst], 0 offen offset:256 sc1\nv_exp_f32 v102, v60\nv_exp_f32 v104, v61\n" \
  "v_exp_f32 v103, v62\nv_exp_f32 v105, v63\nv_cvt_pk_bf16_f32 v102, v102, v104\nv_cvt_pk_bf16_f32 v103, v103, v105\nv_mfma_f32_32x32x4_2b_bf16 v[32:63], v[126:127], v[128:129], 0\nds_read_b32 v126, v131 offset:33600\n" \
  "s_setprio 1\nv_exp_f32 v96, v64\nv_exp_f32 v104, v65\nv_exp_f32 v97, v66\nv_exp_f32 v105, v67\nv_cvt_pk_bf16_f32 v96, v96, v104\n" \
  "v_cvt_pk_bf16_f32 v97, v97, v105\nbuffer_load_dword v238, v134, %[rsrc], s48 offen nt\ns_add_u32 s48, s48, s49\nbuffer_load_dword v239, v134, %[rsrc], s48 offen nt\ns_add_u32 s48, s48, s49\nv_exp_f32 v98, v68\n" \
  "v_exp_f32 v104, v69\nv_exp_f32 v99, v70\nv_exp_f32 v105, v71\nv_cvt_pk_bf16_f32 v98, v98, v104\nv_cvt_pk_bf16_f32 v99, v99, v105\nv_mfma_f32_32x32x16_bf16 v[16:31], v[114:117], v[100:103], v[16:31]\n" \
  "ds_read_b128 v[114:117], v132 offset:24624\nv_exp_f32 v100, v72\nv_exp_f32 v104, v73\nv_exp_f32 v101, v74\nv_exp_f32 v105, v75\nv_cvt_pk_bf16_f32 v100, v100, v104\n" \
  "v_cvt_pk_bf16_f32 v101, v101, v105\ns_waitcnt lgkmcnt(2)\nv_mfma_f32_32x32x16_bf16 v[0:15], v[110:113], v[96:99], v[0:15]\nv_exp_f32 v102, v76\nv_exp_f32 v104, v77\nv_exp_f32 v103, v78\n" \
  "v_exp_f32 v105, v79\nv_cvt_pk_bf16_f32 v102, v102, v104\nv_cvt_pk_bf16_f32 v103, v103, v105\nv_mfma_f32_32x32x16_bf16 v[32:47], v[106:109], v[118:121], v[32:47]\nv_exp_f32 v96, v80\nv_exp_f32 v104, v81\n" \
  "v_exp_f32 v97, v82\nv_exp_f32 v105, v83\nv_cvt_pk_bf16_f32 v96, v96, v104\nv_cvt_pk_bf16_f32 v97, v97, v105\ns_waitcnt lgkmcnt(0)\nv_mfma_f32_32x32x16_bf16 v[0:15], v[114:117], v[100:103], v[0:15]\n" \
  "v_exp_f32 v98, v84\nv_exp_f32 v104, v85\nv_exp_f32 v99, v86\nv_exp_f32 v105, v87\nv_cvt_pk_bf16_f32 v98, v98, v104\nv_cvt_pk_bf16_f32 v99, v99, v105\n" \
  "v_mfma_f32_32x32x16_bf16 v[48:63], v[106:109], v[122:125], v[48:63]\nds_read_b128 v[106:109], v130 offset:33600\nv_exp_f32 v100, v88\nv_exp_f32 v104, v89\nv_exp_f32 v101, v90\nv_exp_f32 v105, v91\n" \
  "v_cvt_pk_bf16_f32 v100, v100, v104\nv_cvt_pk_bf16_f32 v101, v101, v105\nv_mfma_f32_32x32x16_bf16 v[16:31], v[110:113], v[96:99], v[16:31]\nds_read_b128 v[110:113], v132 offset:28800\nv_exp_f32 v102, v92\nv_exp_f32 v104, v93\n" \
  "v_exp_f32 v103, v94\nv_exp_f32 v105, v95\nv_cvt_pk_bf16_f32 v102, v102, v104\nv_cvt_pk_bf16_f32 v103, v103, v105\nv_mfma_f32_32x32x4_2b_bf16 v[64:95], v[126:127], v[128:129], 0\nds_read_b32 v126, v131 offset:38400\n" \
  "v_exp_f32 v96, v32\nv_exp_f32 v104, v33\nv_exp_f32 v97, v34\nv_exp_f32 v105, v35\nv_cvt_pk_bf16_f32 v96, v96, v104\nv_cvt_pk_bf16_f32 v97, v97, v105\n" \
  "v_exp_f32 v98, v36\nv_exp_f32 v104, v37\nv_exp_f32 v99, v38\nv_exp_f32 v105, v39\nv_cvt_pk_bf16_f32 v98, v98, v104\nv_cvt_pk_bf16_f32 v99, v99, v105\n" \
  "v_mfma_f32_32x32x16_bf16 v[16:31], v[114:117], v[100:103], v[16:31]\nds_read_b128 v[114:117], v132 offset:29424\nv_exp_f32 v100, v40\nv_exp_f32 v104, v41\nv_exp_f32 v101, v42\nv_exp_f32 v105, v43\n" \
  "v_cvt_pk_bf16_f32 v100, v100, v104\nv_cvt_pk_bf16_f32 v101, v101, v105\ns_waitcnt lgkmcnt(2)\nv_mfma_f32_32x32x16_bf16 v[0:15], v[110:113], v[96:99], v[0:15]\nv_exp_f32 v102, v44\nv_exp_f32 v104, v45\n" \
  "v_exp_f32 v103, v46\nv_exp_f32 v105, v47\nv_cvt_pk_bf16_f32 v102, v102, v104\nv_cvt_pk_bf16_f32 v103, v103, v105\nv_mfma_f32_32x32x16_bf16 v[64:79], v[106:109], v[118:121], v[64:79]\nv_exp_f32 v96, v48\n" \
  "v_exp_f32 v104, v49\nv_exp_f32 v97, v50\nv_exp_f32 v105, v51\nv_cvt_pk_bf16_f32 v96, v96, v104\nv_cvt_pk_bf16_f32 v97, v97, v105\ns_waitcnt lgkmcnt(0)\n" \
  "v_mfma_f32_32x32x16_bf16 v[0:15], v[114:117], v[100:103], v[0:15]\ns_setprio 0\nv_exp_f32 v98, v52\nv_exp_f32 v104, v53\nv_exp_f32 v99, v54\nv_exp_f32 v105, v55\n" \
  "v_cvt_pk_bf16_f32 v98, v98, v104\nv_cvt_pk_bf16_f32 v99, v99, v105\nv_mfma_f32_32x32x16_bf16 v[80:95], v[106:109], v[122:125], v[80:95]\nds_read_b128 v[106:109], v130 offset:38400\nv_exp_f32 v100, v56\nv_exp_f32 v104, v57\n" \
  "v_exp_f32 v101, v58\nv_exp_f32 v105, v59\nv_cvt_pk_bf16_f32 v100, v100, v104\nv_cvt_pk_bf16_f32 v101, v101, v105\nv_mfma_f32_32x32x16_bf16 v[16:31], v[110:113], v[96:99], v[16:31]\nds_read_b128 v[110:113], v132 offset:33600\n" \
  "v_exp_f32 v102, v60\nv_exp_f32 v104, v61\nv_exp_f32 v103, v62\nv_exp_f32 v105, v63\nv_cvt_pk_bf16_f32 v102, v102, v104\nv_cvt_pk_bf16_f32 v103, v103, v105\n" \
  "v_mfma_f32_32x32x4_2b_bf16 v[32:63], v[126:127], v[128:129], 0\nds_read_b32 v126, v131 offset:43200\ns_waitcnt vmcnt(54)\ns_barrier\ns_setprio 3\nv_exp_f32 v96, v64\n" \
  "v_exp_f32 v104, v65\nv_exp_f32 v97, v66\nv_exp_f32 v105, v67\nv_cvt_pk_bf16_f32 v96, v96, v104\nv_cvt_pk_bf16_f32 v97, v97, v105\ns_add_u32 m0, s41, 32768\n" \
  "s_add_u32 s47, s44, 0\nbuffer_load_dwordx4 v133, %[rwo], s47 offen lds\nv_exp_f32 v98, v68\nv_exp_f32 v104, v69\nv_exp_f32 v99, v70\nv_exp_f32 v105, v71\n" \
  "v_cvt_pk_bf16_f32 v98, v98, v104\nv_cvt_pk_bf16_f32 v99, v99, v105\nv_mfma_f32_32x32x16_bf16 v[16:31], v[114:117], v[100:103], v[16:31]\nds_read_b128 v[114:117], v132 offset:34224\nv_exp_f32 v100, v72\nv_exp_f32 v104, v73\n" \
  "v_exp_f32 v101, v74\nv_exp_f32 v105, v75\nv_cvt_pk_bf16_f32 v100, v100, v104\nv_cvt_pk_bf16_f32 v101, v101, v105\ns_waitcnt lgkmcnt(2)\nv_mfma_f32_32x32x16_bf16 v[0:15], v[110:113], v[96:99], v[0:15]\n" \
  "v_exp_f32 v102, v76\nv_exp_f32 v104, v77\nv_exp_f32 v103, v78\nv_exp_f32 v105, v79\nv_cvt_pk_bf16_f32 v102, v102, v104\nv_cvt_pk_bf16_f32 v103, v103, v105\n" \
  "v_mfma_f32_32x32x16_bf16 v[32:47], v[106:109], v[118:121], v[32:47]\ns_waitcnt vmcnt(14)\nv_perm_b32 v240, v225, v224, s51\nv_perm_b32 v241, v227, v226, s51\nv_perm_b32 v242, v229, v228, s51\nv_perm_b32 v243, v231, v230, s51\n" \
  "buffer_store_dwordx4 v[240:243], v135, %[rdst], 0 offen offset:512 sc1\nv_exp_f32 v96, v80\nv_exp_f32 v104, v81\nv_exp_f32 v97, v82\nv_exp_f32 v105, v83\nv_cvt_pk_bf16_f32 v96, v96, v104\n" \
  "v_cvt_pk_bf16_f32 v97, v97, v105\ns_waitcnt lgkmcnt(0)\nv_mfma_f32_32x32x16_bf16 v[0:15], v[114:117], v[100:103], v[0:15]\ns_add_u32 m0, s41, 40960\ns_add_u32 s47, s44, 8192\nbuffer_load_dwordx4 v133, %[rwo], s47 offen lds\n" \
  "v_exp_f32 v98, v84\nv_exp_f32 v104, v85\nv_exp_f32 v99, v86\nv_exp_f32 v105, v87\nv_cvt_pk_bf16_f32 v98, v98, v104\nv_cvt_pk_bf16_f32 v99, v99, v105\n" \
  "v_mfma_f32_32x32x16_bf16 v[48:63], v[106:109], v[122:125], v[48:63]\nds_read_b128 v[106:109], v130 offset:43200\nv_exp_f32 v100, v88\nv_exp_f32 v104, v89\nv_exp_f32 v101, v90\nv_exp_f32 v105, v91\n" \
  "v_cvt_pk_bf16_f32 v100, v100, v104\nv_cvt_pk_bf16_f32 v101, v101, v105\nv_mfma_f32_32x32x16_bf16 v[16:31], v[110:113], v[96:99], v[16:31]\nds_read_b128 v[110:113], v132 offset:38400\nv_perm_b32 v240, v225, v224, s52\nv_perm_b32 v241, v227, v226, s52\n" \
  "v_perm_b32 v242, v229, v228, s52\nv_perm_b32 v243, v231, v230, s52\nbuffer_store_dwordx4 v[240:243], v244, %[rdst], 0 offen offset:512 sc1\nv_exp_f32 v102, v92\nv_exp_f32 v104, v93\nv_exp_f32 v103, v94\n" \
  "v_exp_f32 v105, v95\nv_cvt_pk_bf16_f32 v102, v102, v104\nv_cvt_pk_bf16_f32 v103, v103, v105\nv_mfma_f32_32x32x4_2b_bf16 v[64:95], v[126:127], v[128:129], 0\nds_read_b32 v126, v131 offset:48000\nv_add_u32 v130, 38400, v130\n" \
  "v_add_u32 v131, 38400, v131\nv_add_u32 v132, 38400, v132\nv_exp_f32 v96, v32\nv_exp_f32 v104, v33\nv_exp_f32 v97, v34\nv_exp_f32 v105, v35\n" \
  "v_cvt_pk_bf16_f32 v96, v96, v104\nv_cvt_pk_bf16_f32 v97, v97, v105\ns_add_u32 m0, s41, 24576\ns_nop 0\nbuffer_load_dwordx4 v245, %[rcode], 0 offen lds\nv_exp_f32 v98, v36\n" \
  "v_exp_f32 v104, v37\nv_exp_f32 v99, v38\nv_exp_f32 v105, v39\nv_cvt_pk_bf16_f32 v98, v98, v104\nv_cvt_pk_bf16_f32 v99, v99, v105\nv_mfma_f32_32x32x16_bf16 v[16:31], v[114:117], v[100:103], v[16:31]\n" \
  "ds_read_b128 v[114:117], v132 offset:624\nv_exp_f32 v100, v40\nv_exp_f32 v104, v41\nv_exp_f32 v101, v42\nv_exp_f32 v105, v43\nv_cvt_pk_bf16_f32 v100, v100, v104\n" \
  "v_cvt_pk_bf16_f32 v101, v101, v105\ns_waitcnt lgkmcnt(2)\nv_mfma_f32_32x32x16_bf16 v[0:15], v[110:113], v[96:99], v[0:15]\nv_exp_f32 v102, v44\nv_exp_f32 v104, v45\nv_exp_f32 v103, v46\n" \
  "v_exp_f32 v105, v47\nv_cvt_pk_bf16_f32 v102, v102, v104\nv_cvt_pk_bf16_f32 v103, v103, v105\nv_mfma_f32_32x32x16_bf16 v[64:79], v[106:109], v[118:121], v[64:79]\ns_mov_b32 m0, s54\ns_nop 0\n" \
  "buffer_load_dwordx4 v133, %[rwo], s53 offen lds\nv_exp_f32 v96, v48\nv_exp_f32 v104, v49\nv_exp_f32 v97, v50\nv_exp_f32 v105, v51\nv_cvt_pk_bf16_f32 v96, v96, v104\n" \
  "v_cvt_pk_bf16_f32 v97, v97, v105\ns_waitcnt lgkmcnt(0)\nv_mfma_f32_32x32x16_bf16 v[0:15], v[114:117], v[100:103], v[0:15]\ns_mov_b32 m0, s56\ns_nop 0\nbuffer_load_dwordx4 v133, %[rmx], s57 offen lds\n" \
  "v_exp_f32 v98, v52\nv_exp_f32 v104, v53\nv_exp_f32 v99, v54\nv_exp_f32 v105, v55\nv_cvt_pk_bf16_f32 v98, v98, v104\nv_cvt_pk_bf16_f32 v99, v99, v105\n" \
  "v_mfma_f32_32x32x16_bf16 v[80:95], v[106:109], v[122:125], v[80:95]\nds_read_b128 v[106:109], v130 offset:9600\nv_exp_f32 v100, v56\nv_exp_f32 v104, v57\nv_exp_f32 v101, v58\nv_exp_f32 v105, v59\n" \
  "v_cvt_pk_bf16_f32 v100, v100, v104\nv_cvt_pk_bf16_f32 v101, v101, v105\nv_mfma_f32_32x32x16_bf16 v[16:31], v[110:113], v[96:99], v[16:31]\nds_read_b128 v[110:113], v132 offset:4800\ns_add_u32 m0, s41, 53248\ns_nop 0\n" \
  "buffer_load_dwordx4 v133, %[rxin], s55 offen lds\nv_exp_f32 v102, v60\nv_exp_f32 v104, v61\nv_exp_f32 v103, v62\nv_exp_f32 v105, v63\nv_cvt_pk_bf16_f32 v102, v102, v104\n" \
  "v_cvt_pk_bf16_f32 v103, v103, v105\nv_mfma_f32_32x32x4_2b_bf16 v[32:63], v[126:127], v[128:129], 0\nds_read_b32 v126, v131 offset:14400\nv_exp_f32 v96, v64\nv_exp_f32 v104, v65\nv_exp_f32 v97, v66\n" \
  "v_exp_f32 v105, v67\nv_cvt_pk_bf16_f32 v96, v96, v104\nv_cvt_pk_bf16_f32 v97, v97, v105\nv_exp_f32 v98, v68\nv_exp_f32 v104, v69\nv_exp_f32 v99, v70\n" \
  "v_exp_f32 v105, v71\nv_cvt_pk_bf16_f32 v98, v98, v104\nv_cvt_pk_bf16_f32 v99, v99, v105\nv_mfma_f32_32x32x16_bf16 v[16:31], v[114:117], v[100:103], v[16:31]\nds_read_b128 v[114:117], v132 offset:5424\nv_exp_f32 v100, v72\n" \
  "v_exp_f32 v104, v73\nv_exp_f32 v101, v74\nv_exp_f32 v105, v75\nv_cvt_pk_bf16_f32 v100, v100, v104\nv_cvt_pk_bf16_f32 v101, v101, v105\ns_waitcnt lgkmcnt(2)\n" \
  "v_mfma_f32_32x32x16_bf16 v[0:15], v[110:113], v[96:99], v[0:15]\nv_exp_f32 v102, v76\nv_exp_f32 v104, v77\nv_exp_f32 v103, v78\nv_exp_f32 v105, v79\nv_cvt_pk_bf16_f32 v102, v102, v104\n" \
  "v_cvt_pk_bf16_f32 v103, v103, v105\nv_mfma_f32_32x32x16_bf16 v[32:47], v[106:109], v[118:121], v[32:47]\nv_exp_f32 v96, v80\nv_exp_f32 v104, v81\nv_exp_f32 v97, v82\nv_exp_f32 v105, v83\n" \
  "v_cvt_pk_bf16_f32 v96, v96, v104\nv_cvt_pk_bf16_f32 v97, v97, v105\ns_waitcnt lgkmcnt(0)\nv_mfma_f32_32x32x16_bf16 v[0:15], v[114:117], v[100:103], v[0:15]\nv_exp_f32 v98, v84\nv_exp_f32 v104, v85\n" \
  "v_exp_f32 v99, v86\nv_exp_f32 v105, v87\nv_cvt_pk_bf16_f32 v98, v98, v104\nv_cvt_pk_bf16_f32 v99, v99, v105\nv_mfma_f32_32x32x16_bf16 v[48:63], v[106:109], v[122:125], v[48:63]\nds_read_b128 v[106:109], v130 offset:14400\n" \
  "v_exp_f32 v100, v88\nv_exp_f32 v104, v89\nv_exp_f32 v101, v90\nv_exp_f32 v105, v91\nv_cvt_pk_bf16_f32 v100, v100, v104\nv_cvt_pk_bf16_f32 v101, v101, v105\n" \
  "v_mfma_f32_32x32x16_bf16 v[16:31], v[110:113], v[96:99], v[16:31]\nds_read_b128 v[110:113], v132 offset:9600\nv_exp_f32 v102, v92\nv_exp_f32 v104, v93\nv_exp_f32 v103, v94\nv_exp_f32 v105, v95\n" \
  "v_cvt_pk_bf16_f32 v102, v102, v104\nv_cvt_pk_bf16_f32 v103, v103, v105\nv_mfma_f32_32x32x4_2b_bf16 v[64:95], v[126:127], v[128:129], 0\nds_read_b32 v126, v131 offset:19200\ns_setprio 2\nv_exp_f32 v96, v32\n" \
  "v_exp_f32 v104, v33\nv_exp_f32 v97, v34\nv_exp_f32 v105, v35\nv_cvt_pk_bf16_f32 v96, v96, v104\nv_cvt_pk_bf16_f32 v97, v97, v105\nv_exp_f32 v98, v36\n" \
  "v_exp_f32 v104, v37\nv_exp_f32 v99, v38\nv_exp_f32 v105, v39\nv_cvt_pk_bf16_f32 v98, v98, v104\nv_cvt_pk_bf16_f32 v99, v99, v105\nv_mfma_f32_32x32x16_bf16 v[16:31], v[114:117], v[100:103], v[16:31]\n" \
  "ds_read_b128 v[114:117], v132 offset:10224\nv_exp_f32 v100, v40\nv_exp_f32 v104, v41\nv_exp_f32 v101, v42\nv_exp_f32 v105, v43\nv_cvt_pk_bf16_f32 v100, v100, v104\n" \
  "v_cvt_pk_bf16_f32 v101, v101, v105\ns_waitcnt lgkmcnt(2)\nv_mfma_f32_32x32x16_bf16 v[0:15], v[110:113], v[96:99], v[0:15]\nv_exp_f32 v102, v44\nv_exp_f32 v104, v45\nv_exp_f32 v103, v46\n" \
  "v_exp_f32 v105, v47\nv_cvt_pk_bf16_f32 v102, v102, v104\nv_cvt_pk_bf16_f32 v103, v103, v105\nv_mfma_f32_32x32x16_bf16 v[64:79], v[106:109], v[118:121], v[64:79]\ns_waitcnt vmcnt(8)\nv_perm_b32 v240, v233, v232, s51\n" \
  "v_perm_b32 v241, v235, v234, s51\nv_perm_b32 v242, v237, v236, s51\nv_perm_b32 v243, v239, v238, s51\nbuffer_store_dwordx4 v[240:243], v135, %[rdst], 0 offen offset:768 sc1\nv_exp_f32 v96, v48\nv_exp_f32 v104, v49\n" \
  "v_exp_f32 v97, v50\nv_exp_f32 v105, v51\nv_cvt_pk_bf16_f32 v96, v96, v104\nv_cvt_pk_bf16_f32 v97, v97, v105\ns_waitcnt lgkmcnt(0)\nv_mfma_f32_32x32x16_bf16 v[0:15], v[114:117], v[100:103], v[0:15]\n" \
  "v_exp_f32 v98, v52\nv_exp_f32 v104, v53\nv_exp_f32 v99, v54\nv_exp_f32 v105, v55\nv_cvt_pk_bf16_f32 v98, v98, v104\nv_cvt_pk_bf16_f32 v99, v99, v105\n" \
  "v_mfma_f32_32x32x16_bf16 v[80:95], v[106:109], v[122:125], v[80:95]\nds_read_b128 v[106:109], v130 offset:19200\nv_exp_f32 v100, v56\nv_exp_f32 v104, v57\nv_exp_f32 v101, v58\nv_exp_f32 v105, v59\n" \
  "v_cvt_pk_bf16_f32 v100, v100, v104\nv_cvt_pk_bf16_f32 v101, v101, v105\nv_mfma_f32_32x32x16_bf16 v[16:31], v[110:113], v[96:99], v[16:31]\nds_read_b128 v[110:113], v132 offset:14400\nv_perm_b32 v240, v233, v232, s52\nv_perm_b32 v241, v235, v234, s52\n" \
  "v_perm_b32 v242, v237, v236, s52\nv_perm_b32 v243, v239, v238, s52\nbuffer_store_dwordx4 v[240:243], v244, %[rdst], 0 offen offset:768 sc1\nv_exp_f32 v102, v60\nv_exp_f32 v104, v61\nv_exp_f32 v103, v62\n" \
  "v_exp_f32 v105, v63\nv_cvt_pk_bf16_f32 v102, v102, v104\nv_cvt_pk_bf16_f32 v103, v103, v105\nv_mfma_f32_32x32x4_2b_bf16 v[32:63], v[126:127], v[128:129], 0\nds_read_b32 v126, v131 offset:24000\nv_exp_f32 v96, v64\n" \
  "v_exp_f32 v104, v65\nv_exp_f32 v97, v66\nv_exp_f32 v105, v67\nv_cvt_pk_bf16_f32 v96, v96, v104\nv_cvt_pk_bf16_f32 v97, v97, v105\nv_exp_f32 v98, v68\n" \
  "v_exp_f32 v104, v69\nv_exp_f32 v99, v70\nv_exp_f32 v105, v71\nv_cvt_pk_bf16_f32 v98, v98, v104\nv_cvt_pk_bf16_f32 v99, v99, v105\nv_mfma_f32_32x32x16_bf16 v[16:31], v[114:117], v[100:103], v[16:31]\n" \
  "ds_read_b128 v[114:117], v132 offset:15024\nv_exp_f32 v100, v72\nv_exp_f32 v104, v73\nv_exp_f32 v101, v74\nv_exp_f32 v105, v75\nv_cvt_pk_bf16_f32 v100, v100, v104\n" \
  "v_cvt_pk_bf16_f32 v101, v101, v105\ns_waitcnt lgkmcnt(2)\nv_mfma_f32_32x32x16_bf16 v[0:15], v[110:113], v[96:99], v[0:15]\nv_exp_f32 v102, v76\nv_exp_f32 v104, v77\nv_exp_f32 v103, v78\n" \
  "v_exp_f32 v105, v79\nv_cvt_pk_bf16_f32 v102, v102, v104\nv_cvt_pk_bf16_f32 v103, v103, v105\nv_mfma_f32_32x32x16_bf16 v[32:47], v[106:109], v[118:121], v[32:47]\nv_exp_f32 v96, v80\nv_exp_f32 v104, v81\n" \
  "v_exp_f32 v97, v82\nv_exp_f32 v105, v83\nv_cvt_pk_bf16_f32 v96, v96, v104\nv_cvt_pk_bf16_f32 v97, v97, v105\ns_waitcnt lgkmcnt(0)\nv_mfma_f32_32x32x16_bf16 v[0:15], v[114:117], v[100:103], v[0:15]\n" \
  "v_exp_f32 v98, v84\nv_exp_f32 v104, v85\nv_exp_f32 v99, v86\nv_exp_f32 v105, v87\nv_cvt_pk_bf16_f32 v98, v98, v104\nv_cvt_pk_bf16_f32 v99, v99, v105\n" \
  "v_mfma_f32_32x32x16_bf16 v[48:63], v[106:109], v[122:125], v[48:63]\nds_read_b128 v[106:109], v130 offset:24000\nv_exp_f32 v100, v88\nv_exp_f32 v104, v89\nv_exp_f32 v101, v90\nv_exp_f32 v105, v91\n" \
  "v_cvt_pk_bf16_f32 v100, v100, v104\nv_cvt_pk_bf16_f32 v101, v101, v105\nv_mfma_f32_32x32x16_bf16 v[16:31], v[110:113], v[96:99], v[16:31]\nds_read_b128 v[110:113], v132 offset:19200\nv_exp_f32 v102, v92\nv_exp_f32 v104, v93\n" \
  "v_exp_f32 v103, v94\nv_exp_f32 v105, v95\nv_cvt_pk_bf16_f32 v102, v102, v104\nv_cvt_pk_bf16_f32 v103, v103, v105\nv_mfma_f32_32x32x4_2b_bf16 v[64:95], v[126:127], v[128:129], 0\nds_read_b32 v126, v131 offset:28800\n" \
  "v_exp_f32 v96, v32\nv_exp_f32 v104, v33\nv_exp_f32 v97, v34\nv_exp_f32 v105, v35\nv_cvt_pk_bf16_f32 v96, v96, v104\nv_cvt_pk_bf16_f32 v97, v97, v105\n" \
  "v_exp_f32 v98, v36\nv_exp_f32 v104, v37\nv_exp_f32 v99, v38\nv_exp_f32 v105, v39\nv_cvt_pk_bf16_f32 v98, v98, v104\nv_cvt_pk_bf16_f32 v99, v99, v105\n" \
  "v_mfma_f32_32x32x16_bf16 v[16:31], v[114:117], v[100:103], v[16:31]\nds_read_b128 v[114:117], v132 offset:19824\nv_exp_f32 v100, v40\nv_exp_f32 v104, v41\nv_exp_f32 v101, v42\nv_exp_f32 v105, v43\n" \
  "v_cvt_pk_bf16_f32 v100, v100, v104\nv_cvt_pk_bf16_f32 v101, v101, v105\ns_waitcnt lgkmcnt(2)\nv_mfma_f32_32x32x16_bf16 v[0:15], v[110:113], v[96:99], v[0:15]\nv_exp_f32 v102, v44\nv_exp_f32 v104, v45\n" \
  "v_exp_f32 v103, v46\nv_exp_f32 v105, v47\nv_cvt_pk_bf16_f32 v102, v102, v104\nv_cvt_pk_bf16_f32 v103, v103, v105\nv_mfma_f32_32x32x16_bf16 v[64:79], v[106:109], v[118:121], v[64:79]\nv_exp_f32 v96, v48\n" \
  "v_exp_f32 v104, v49\nv_exp_f32 v97, v50\nv_exp_f32 v105, v51\nv_cvt_pk_bf16_f32 v96, v96, v104\nv_cvt_pk_bf16_f32 v97, v97, v105\ns_waitcnt lgkmcnt(0)\n" \
  "v_mfma_f32_32x32x16_bf16 v[0:15], v[114:117], v[100:103], v[0:15]\nv_exp_f32 v98, v52\nv_exp_f32 v104, v53\nv_exp_f32 v99, v54\nv_exp_f32 v105, v55\nv_cvt_pk_bf16_f32 v98, v98, v104\n" \
  "v_cvt_pk_bf16_f32 v99, v99, v105\nv_mfma_f32_32x32x16_bf16 v[80:95], v[106:109], v[122:125], v[80:95]\nds_read_b128 v[106:109], v130 offset:28800\nv_exp_f32 v100, v56\nv_exp_f32 v104, v57\nv_exp_f32 v101, v58\n" \
  "v_exp_f32 v105, v59\nv_cvt_pk_bf16_f32 v100, v100, v104\nv_cvt_pk_bf16_f32 v101, v101, v105\nv_mfma_f32_32x32x16_bf16 v[16:31], v[110:113], v[96:99], v[16:31]\nds_read_b128 v[110:113], v132 offset:24000\nv_exp_f32 v102, v60\n" \
  "v_exp_f32 v104, v61\nv_exp_f32 v103, v62\nv_exp_f32 v105, v63\nv_cvt_pk_bf16_f32 v102, v102, v104\nv_cvt_pk_bf16_f32 v103, v103, v105\nv_mfma_f32_32x32x4_2b_bf16 v[32:63], v[126:127], v[128:129], 0\n" \
  "ds_read_b32 v126, v131 offset:33600\ns_waitcnt vmcnt(0)\ns_mov_b64 exec, 1\nv_mov_b32 v240, 0\nv_mov_b32 v241, 1\nglobal_atomic_add v240, v241, %[wcnt]\n" \
  "s_mov_b64 exec, -1\nv_exp_f32 v96, v64\nv_exp_f32 v104, v65\nv_exp_f32 v97, v66\nv_exp_f32 v105, v67\nv_cvt_pk_bf16_f32 v96, v96, v104\n" \
  "v_cvt_pk_bf16_f32 v97, v97, v105\nv_exp_f32 v98, v68\nv_exp_f32 v104, v69\nv_exp_f32 v99, v70\nv_exp_f32 v105, v71\nv_cvt_pk_bf16_f32 v98, v98, v104\n" \
  "v_cvt_pk_bf16_f32 v99, v99, v105\nv_mfma_f32_32x32x16_bf16 v[16:31], v[114:117], v[100:103], v[16:31]\nds_read_b128 v[114:117], v132 offset:24624\ns_setprio 1\nv_exp_f32 v100, v72\nv_exp_f32 v104, v73\n" \
  "v_exp_f32 v101, v74\nv_exp_f32 v105, v75\nv_cvt_pk_bf16_f32 v100, v100, v104\nv_cvt_pk_bf16_f32 v101, v101, v105\ns_waitcnt lgkmcnt(2)\nv_mfma_f32_32x32x16_bf16 v[0:15], v[110:113], v[96:99], v[0:15]\n" \
  "v_exp_f32 v102, v76\nv_exp_f32 v104, v77\nv_exp_f32 v103, v78\nv_exp_f32 v105, v79\nv_cvt_pk_bf16_f32 v102, v102, v104\nv_cvt_pk_bf16_f32 v103, v103, v105\n" \
  "v_mfma_f32_32x32x16_bf16 v[32:47], v[106:109], v[118:121], v[32:47]\nv_exp_f32 v96, v80\nv_exp_f32 v104, v81\nv_exp_f32 v97, v82\nv_exp_f32 v105, v83\nv_cvt_pk_bf16_f32 v96, v96, v104\n" \
  "v_cvt_pk_bf16_f32 v97, v97, v105\ns_waitcnt lgkmcnt(0)\nv_mfma_f32_32x32x16_bf16 v[0:15], v[114:117], v[100:103], v[0:15]\nv_exp_f32 v98, v84\nv_exp_f32 v104, v85\nv_exp_f32 v99, v86\n" \
  "v_exp_f32 v105, v87\nv_cvt_pk_bf16_f32 v98, v98, v104\nv_cvt_pk_bf16_f32 v99, v99, v105\nv_mfma_f32_32x32x16_bf16 v[48:63], v[106:109], v[122:125], v[48:63]\nds_read_b128 v[106:109], v130 offset:33600\nv_exp_f32 v100, v88\n" \
  "v_exp_f32 v104, v89\nv_exp_f32 v101, v90\nv_exp_f32 v105, v91\nv_cvt_pk_bf16_f32 v100, v100, v104\nv_cvt_pk_bf16_f32 v101, v101, v105\nv_mfma_f32_32x32x16_bf16 v[16:31], v[110:113], v[96:99], v[16:31]\n" \
  "ds_read_b128 v[110:113], v132 offset:28800\nv_exp_f32 v102, v92\nv_exp_f32 v104, v93\nv_exp_f32 v103, v94\nv_exp_f32 v105, v95\nv_cvt_pk_bf16_f32 v102, v102, v104\n" \
  "v_cvt_pk_bf16_f32 v103, v103, v105\nv_mfma_f32_32x32x4_2b_bf16 v[64:95], v[126:127], v[128:129], 0\nds_read_b32 v126, v131 offset:38400\nv_exp_f32 v96, v32\nv_exp_f32 v104, v33\nv_exp_f32 v97, v34\n" \
  "v_exp_f32 v105, v35\nv_cvt_pk_bf16_f32 v96, v96, v104\nv_cvt_pk_bf16_f32 v97, v97, v105\nv_exp_f32 v98, v36\nv_exp_f32 v104, v37\nv_exp_f32 v99, v38\n" \
  "v_exp_f32 v105, v39\nv_cvt_pk_bf16_f32 v98, v98, v104\nv_cvt_pk_bf16_f32 v99, v99, v105\nv_mfma_f32_32x32x16_bf16 v[16:31], v[114:117], v[100:103], v[16:31]\nds_read_b128 v[114:117], v132 offset:29424\nv_exp_f32 v100, v40\n" \
  "v_exp_f32 v104, v41\nv_exp_f32 v101, v42\nv_exp_f32 v105, v43\nv_cvt_pk_bf16_f32 v100, v100, v104\nv_cvt_pk_bf16_f32 v101, v101, v105\ns_waitcnt lgkmcnt(2)\n" \
  "v_mfma_f32_32x32x16_bf16 v[0:15], v[110:113], v[96:99], v[0:15]\nv_exp_f32 v102, v44\nv_exp_f32 v104, v45\nv_exp_f32 v103, v46\nv_exp_f32 v105, v47\nv_cvt_pk_bf16_f32 v102, v102, v104\n" \
  "v_cvt_pk_bf16_f32 v103, v103, v105\nv_mfma_f32_32x32x16_bf16 v[64:79], v[106:109], v[118:121], v[64:79]\nv_exp_f32 v96, v48\nv_exp_f32 v104, v49\nv_exp_f32 v97, v50\nv_exp_f32 v105, v51\n" \
  "v_cvt_pk_bf16_f32 v96, v96, v104\nv_cvt_pk_bf16_f32 v97, v97, v105\ns_waitcnt lgkmcnt(0)\nv_mfma_f32_32x32x16_bf16 v[0:15], v[114:117], v[100:103], v[0:15]\nv_exp_f32 v98, v52\nv_exp_f32 v104, v53\n" \
  "v_exp_f32 v99, v54\nv_exp_f32 v105, v55\nv_cvt_pk_bf16_f32 v98, v98, v104\nv_cvt_pk_bf16_f32 v99, v99, v105\nv_mfma_f32_32x32x16_bf16 v[80:95], v[106:109], v[122:125], v[80:95]\nds_read_b128 v[106:109], v130 offset:38400\n" \
  "v_exp_f32 v100, v56\nv_exp_f32 v104, v57\nv_exp_f32 v101, v58\nv_exp_f32 v105, v59\nv_cvt_pk_bf16_f32 v100, v100, v104\nv_cvt_pk_bf16_f32 v101, v101, v105\n" \
  "v_mfma_f32_32x32x16_bf16 v[16:31], v[110:113], v[96:99], v[16:31]\nds_read_b128 v[110:113], v132 offset:33600\nv_exp_f32 v102, v60\nv_exp_f32 v104, v61\nv_exp_f32 v103, v62\nv_exp_f32 v105, v63\n" \
  "v_cvt_pk_bf16_f32 v102, v102, v104\nv_cvt_pk_bf16_f32 v103, v103, v105\nv_mfma_f32_32x32x4_2b_bf16 v[32:63], v[126:127], v[128:129], 0\nds_read_b32 v126, v131 offset:43200\nv_exp_f32 v96, v64\nv_exp_f32 v104, v65\n" \
  "v_exp_f32 v97, v66\nv_exp_f32 v105, v67\nv_cvt_pk_bf16_f32 v96, v96, v104\nv_cvt_pk_bf16_f32 v97, v97, v105\nv_exp_f32 v98, v68\nv_exp_f32 v104, v69\n" \
  "v_exp_f32 v99, v70\nv_exp_f32 v105, v71\nv_cvt_pk_bf16_f32 v98, v98, v104\nv_cvt_pk_bf16_f32 v99, v99, v105\nv_mfma_f32_32x32x16_bf16 v[16:31], v[114:117], v[100:103], v[16:31]\nds_read_b128 v[114:117], v132 offset:34224\n" \
  "v_exp_f32 v100, v72\nv_exp_f32 v104, v73\nv_exp_f32 v101, v74\nv_exp_f32 v105, v75\nv_cvt_pk_bf16_f32 v100, v100, v104\nv_cvt_pk_bf16_f32 v101, v101, v105\n" \
  "s_waitcnt lgkmcnt(2)\nv_mfma_f32_32x32x16_bf16 v[0:15], v[110:113], v[96:99], v[0:15]\nv_exp_f32 v102, v76\nv_exp_f32 v104, v77\nv_exp_f32 v103, v78\nv_exp_f32 v105, v79\n" \
  "v_cvt_pk_bf16_f32 v102, v102, v104\nv_cvt_pk_bf16_f32 v103, v103, v105\nv_mfma_f32_32x32x16_bf16 v[32:47], v[106:109], v[118:121], v[32:47]\nv_exp_f32 v96, v80\nv_exp_f32 v104, v81\nv_exp_f32 v97, v82\n" \
  "v_exp_f32 v105, v83\nv_cvt_pk_bf16_f32 v96, v96, v104\nv_cvt_pk_bf16_f32 v97, v97, v105\ns_waitcnt lgkmcnt(0)\nv_mfma_f32_32x32x16_bf16 v[0:15], v[114:117], v[100:103], v[0:15]\nv_exp_f32 v98, v84\n" \
  "v_exp_f32 v104, v85\nv_exp_f32 v99, v86\nv_exp_f32 v105, v87\nv_cvt_pk_bf16_f32 v98, v98, v104\nv_cvt_pk_bf16_f32 v99, v99, v105\nv_mfma_f32_32x32x16_bf16 v[48:63], v[106:109], v[122:125], v[48:63]\n" \
  "ds_read_b128 v[106:109], v130 offset:43200\nv_exp_f32 v100, v88\nv_exp_f32 v104, v89\nv_exp_f32 v101, v90\nv_exp_f32 v105, v91\nv_cvt_pk_bf16_f32 v100, v100, v104\n" \
  "v_cvt_pk_bf16_f32 v101, v101, v105\nv_mfma_f32_32x32x16_bf16 v[16:31], v[110:113], v[96:99], v[16:31]\nds_read_b128 v[110:113], v132 offset:38400\nv_exp_f32 v102, v92\nv_exp_f32 v104, v93\nv_exp_f32 v103, v94\n" \
  "v_exp_f32 v105, v95\nv_cvt_pk_bf16_f32 v102, v102, v104\nv_cvt_pk_bf16_f32 v103, v103, v105\nv_mfma_f32_32x32x4_2b_bf16 v[64:95], v[126:127], v[128:129], 0\nds_read_b32 v126, v131 offset:48000\nv_add_u32 v130, 38400, v130\n" \
  "v_add_u32 v131, 38400, v131\nv_add_u32 v132, 38400, v132\nv_exp_f32 v96, v32\nv_exp_f32 v104, v33\nv_exp_f32 v97, v34\nv_exp_f32 v105, v35\n" \
  "v_cvt_pk_bf16_f32 v96, v96, v104\nv_cvt_pk_bf16_f32 v97, v97, v105\nv_exp_f32 v98, v36\nv_exp_f32 v104, v37\nv_exp_f32 v99, v38\nv_exp_f32 v105, v39\n" \
  "v_cvt_pk_bf16_f32 v98, v98, v104\nv_cvt_pk_bf16_f32 v99, v99, v105\nv_mfma_f32_32x32x16_bf16 v[16:31], v[114:117], v[100:103], v[16:31]\nds_read_b128 v[114:117], v132 offset:624\nv_exp_f32 v100, v40\nv_exp_f32 v104, v41\n" \
  "v_exp_f32 v101, v42\nv_exp_f32 v105, v43\nv_cvt_pk_bf16_f32 v100, v100, v104\nv_cvt_pk_bf16_f32 v101, v101, v105\ns_waitcnt lgkmcnt(2)\nv_mfma_f32_32x32x16_bf16 v[0:15], v[110:113], v[96:99], v[0:15]\n" \
  "v_exp_f32 v102, v44\nv_exp_f32 v104, v45\nv_exp_f32 v103, v46\nv_exp_f32 v105, v47\nv_cvt_pk_bf16_f32 v102, v102, v104\nv_cvt_pk_bf16_f32 v103, v103, v105\n" \
  "v_mfma_f32_32x32x16_bf16 v[64:79], v[106:109], v[118:121], v[64:79]\nv_exp_f32 v96, v48\nv_exp_f32 v104, v49\nv_exp_f32 v97, v50\nv_exp_f32 v105, v51\nv_cvt_pk_bf16_f32 v96, v96, v104\n" \
  "v_cvt_pk_bf16_f32 v97, v97, v105\ns_waitcnt lgkmcnt(0)\nv_mfma_f32_32x32x16_bf16 v[0:15], v[114:117], v[100:103], v[0:15]\nv_exp_f32 v98, v52\nv_exp_f32 v104, v53\nv_exp_f32 v99, v54\n" \
  "v_exp_f32 v105, v55\nv_cvt_pk_bf16_f32 v98, v98, v104\nv_cvt_pk_bf16_f32 v99, v99, v105\nv_mfma_f32_32x32x16_bf16 v[80:95], v[106:109], v[122:125], v[80:95]\nds_read_b128 v[106:109], v130 offset:9600\nv_exp_f32 v100, v56\n" \
  "v_exp_f32 v104, v57\nv_exp_f32 v101, v58\nv_exp_f32 v105, v59\nv_cvt_pk_bf16_f32 v100, v100, v104\nv_cvt_pk_bf16_f32 v101, v101, v105\nv_mfma_f32_32x32x16_bf16 v[16:31], v[110:113], v[96:99], v[16:31]\n" \
  "ds_read_b128 v[110:113], v132 offset:4800\nv_exp_f32 v102, v60\nv_exp_f32 v104, v61\nv_exp_f32 v103, v62\nv_exp_f32 v105, v63\nv_cvt_pk_bf16_f32 v102, v102, v104\n" \
  "v_cvt_pk_bf16_f32 v103, v103, v105\nv_mfma_f32_32x32x4_2b_bf16 v[32:63], v[126:127], v[128:129], 0\nds_read_b32 v126, v131 offset:14400\nv_exp_f32 v96, v64\nv_exp_f32 v104, v65\nv_exp_f32 v97, v66\n" \
  "v_exp_f32 v105, v67\nv_cvt_pk_bf16_f32 v96, v96, v104\nv_cvt_pk_bf16_f32 v97, v97, v105\nv_exp_f32 v98, v68\nv_exp_f32 v104, v69\nv_exp_f32 v99, v70\n" \
  "v_exp_f32 v105, v71\nv_cvt_pk_bf16_f32 v98, v98, v104\nv_cvt_pk_bf16_f32 v99, v99, v105\nv_mfma_f32_32x32x16_bf16 v[16:31], v[114:117], v[100:103], v[16:31]\nds_read_b128 v[114:117], v132 offset:5424\nv_exp_f32 v100, v72\n" \
  "v_exp_f32 v104, v73\nv_exp_f32 v101, v74\nv_exp_f32 v105, v75\nv_cvt_pk_bf16_f32 v100, v100, v104\nv_cvt_pk_bf16_f32 v101, v101, v105\ns_waitcnt lgkmcnt(2)\n" \
  "v_mfma_f32_32x32x16_bf16 v[0:15], v[110:113], v[96:99], v[0:15]\nv_exp_f32 v102, v76\nv_exp_f32 v104, v77\nv_exp_f32 v103, v78\nv_exp_f32 v105, v79\nv_cvt_pk_bf16_f32 v102, v102, v104\n" \
  "v_cvt_pk_bf16_f32 v103, v103, v105\nv_mfma_f32_32x32x16_bf16 v[32:47], v[106:109], v[118:121], v[32:47]\nv_exp_f32 v96, v80\nv_exp_f32 v104, v81\nv_exp_f32 v97, v82\nv_exp_f32 v105, v83\n" \
  "v_cvt_pk_bf16_f32 v96, v96, v104\nv_cvt_pk_bf16_f32 v97, v97, v105\ns_waitcnt lgkmcnt(0)\nv_mfma_f32_32x32x16_bf16 v[0:15], v[114:117], v[100:103], v[0:15]\nv_exp_f32 v98, v84\nv_exp_f32 v104, v85\n" \
  "v_exp_f32 v99, v86\nv_exp_f32 v105, v87\nv_cvt_pk_bf16_f32 v98, v98, v104\nv_cvt_pk_bf16_f32 v99, v99, v105\nv_mfma_f32_32x32x16_bf16 v[48:63], v[106:109], v[122:125], v[48:63]\nds_read_b128 v[106:109], v130 offset:14400\n" \
  "v_exp_f32 v100, v88\nv_exp_f32 v104, v89\nv_exp_f32 v101, v90\nv_exp_f32 v105, v91\nv_cvt_pk_bf16_f32 v100, v100, v104\nv_cvt_pk_bf16_f32 v101, v101, v105\n" \
  "v_mfma_f32_32x32x16_bf16 v[16:31], v[110:113], v[96:99], v[16:31]\nds_read_b128 v[110:113], v132 offset:9600\nv_exp_f32 v102, v92\nv_exp_f32 v104, v93\nv_exp_f32 v103, v94\nv_exp_f32 v105, v95\n" \
  "v_cvt_pk_bf16_f32 v102, v102, v104\nv_cvt_pk_bf16_f32 v103, v103, v105\nv_mfma_f32_32x32x4_2b_bf16 v[64:95], v[126:127], v[128:129], 0\nds_read_b32 v126, v131 offset:19200\ns_cmp_lg_u32 s58, 0\ns_cbranch_scc1 8f\n" \
  "v_lshlrev_b32 v240, 3, v133\nv_and_b32 v240, 0x1f80, v240\nglobal_load_dword v241, v240, %[wall] sc1\n8:\nv_exp_f32 v96, v32\nv_exp_f32 v104, v33\n" \
  "v_exp_f32 v97, v34\nv_exp_f32 v105, v35\nv_cvt_pk_bf16_f32 v96, v96, v104\nv_cvt_pk_bf16_f32 v97, v97, v105\nv_exp_f32 v98, v36\nv_exp_f32 v104, v37\n" \
  "v_exp_f32 v99, v38\nv_exp_f32 v105, v39\nv_cvt_pk_bf16_f32 v98, v98, v104\nv_cvt_pk_bf16_f32 v99, v99, v105\nv_mfma_f32_32x32x16_bf16 v[16:31], v[114:117], v[100:103], v[16:31]\nds_read_b128 v[114:117], v132 offset:10224\n" \
  "v_exp_f32 v100, v40\nv_exp_f32 v104, v41\nv_exp_f32 v101, v42\nv_exp_f32 v105, v43\nv_cvt_pk_bf16_f32 v100, v100, v104\nv_cvt_pk_bf16_f32 v101, v101, v105\n" \
  "s_waitcnt lgkmcnt(2)\nv_mfma_f32_32x32x16_bf16 v[0:15], v[110:113], v[96:99], v[0:15]\nv_exp_f32 v102, v44\nv_exp_f32 v104, v45\nv_exp_f32 v103, v46\nv_exp_f32 v105, v47\n" \
  "v_cvt_pk_bf16_f32 v102, v102, v104\nv_cvt_pk_bf16_f32 v103, v103, v105\nv_mfma_f32_32x32x16_bf16 v[64:79], v[106:109], v[118:121], v[64:79]\nv_exp_f32 v96, v48\nv_exp_f32 v104, v49\nv_exp_f32 v97, v50\n" \
  "v_exp_f32 v105, v51\nv_cvt_pk_bf16_f32 v96, v96, v104\nv_cvt_pk_bf16_f32 v97, v97, v105\ns_waitcnt lgkmcnt(0)\nv_mfma_f32_32x32x16_bf16 v[0:15], v[114:117], v[100:103], v[0:15]\nv_exp_f32 v98, v52\n" \
  "v_exp_f32 v104, v53\nv_exp_f32 v99, v54\nv_exp_f32 v105, v55\nv_cvt_pk_bf16_f32 v98, v98, v104\nv_cvt_pk_bf16_f32 v99, v99, v105\nv_mfma_f32_32x32x16_bf16 v[80:95], v[106:109], v[122:125], v[80:95]\n" \
  "ds_read_b128 v[106:109], v130 offset:19200\nv_exp_f32 v100, v56\nv_exp_f32 v104, v57\nv_exp_f32 v101, v58\nv_exp_f32 v105, v59\nv_cvt_pk_bf16_f32 v100, v100, v104\n" \
  "v_cvt_pk_bf16_f32 v101, v101, v105\nv_mfma_f32_32x32x16_bf16 v[16:31], v[110:113], v[96:99], v[16:31]\nds_read_b128 v[110:113], v132 offset:14400\nv_exp_f32 v102, v60\nv_exp_f32 v104, v61\nv_exp_f32 v103, v62\n" \
  "v_exp_f32 v105, v63\nv_cvt_pk_bf16_f32 v102, v102, v104\nv_cvt_pk_bf16_f32 v103, v103, v105\nv_mfma_f32_32x32x4_2b_bf16 v[32:63], v[126:127], v[128:129], 0\nds_read_b32 v126, v131 offset:24000\nv_exp_f32 v96, v64\n" \
  "v_exp_f32 v104, v65\nv_exp_f32 v97, v66\nv_exp_f32 v105, v67\nv_cvt_pk_bf16_f32 v96, v96, v104\nv_cvt_pk_bf16_f32 v97, v97, v105\nv_exp_f32 v98, v68\n" \
  "v_exp_f32 v104, v69\nv_exp_f32 v99, v70\nv_exp_f32 v105, v71\nv_cvt_pk_bf16_f32 v98, v98, v104\nv_cvt_pk_bf16_f32 v99, v99, v105\nv_mfma_f32_32x32x16_bf16 v[16:31], v[114:117], v[100:103], v[16:31]\n" \
  "ds_read_b128 v[114:117], v132 offset:15024\nv_exp_f32 v100, v72\nv_exp_f32 v104, v73\nv_exp_f32 v101, v74\nv_exp_f32 v105, v75\nv_cvt_pk_bf16_f32 v100, v100, v104\n" \
  "v_cvt_pk_bf16_f32 v101, v101, v105\ns_waitcnt lgkmcnt(2)\nv_mfma_f32_32x32x16_bf16 v[0:15], v[110:113], v[96:99], v[0:15]\nv_exp_f32 v102, v76\nv_exp_f32 v104, v77\nv_exp_f32 v103, v78\n" \
  "v_exp_f32 v105, v79\nv_cvt_pk_bf16_f32 v102, v102, v104\nv_cvt_pk_bf16_f32 v103, v103, v105\nv_mfma_f32_32x32x16_bf16 v[32:47], v[106:109], v[118:121], v[32:47]\nv_exp_f32 v96, v80\nv_exp_f32 v104, v81\n" \
  "v_exp_f32 v97, v82\nv_exp_f32 v105, v83\nv_cvt_pk_bf16_f32 v96, v96, v104\nv_cvt_pk_bf16_f32 v97, v97, v105\ns_waitcnt lgkmcnt(0)\nv_mfma_f32_32x32x16_bf16 v[0:15], v[114:117], v[100:103], v[0:15]\n" \
  "v_exp_f32 v98, v84\nv_exp_f32 v104, v85\nv_exp_f32 v99, v86\nv_exp_f32 v105, v87\nv_cvt_pk_bf16_f32 v98, v98, v104\nv_cvt_pk_bf16_f32 v99, v99, v105\n" \
  "v_mfma_f32_32x32x16_bf16 v[48:63], v[106:109], v[122:125], v[48:63]\nds_read_b128 v[106:109], v130 offset:24000\nv_exp_f32 v100, v88\nv_exp_f32 v104, v89\nv_exp_f32 v101, v90\nv_exp_f32 v105, v91\n" \
  "v_cvt_pk_bf16_f32 v100, v100, v104\nv_cvt_pk_bf16_f32 v101, v101, v105\nv_mfma_f32_32x32x16_bf16 v[16:31], v[110:113], v[96:99], v[16:31]\nds_read_b128 v[110:113], v132 offset:19200\nv_exp_f32 v102, v92\nv_exp_f32 v104, v93\n" \
  "v_exp_f32 v103, v94\nv_exp_f32 v105, v95\nv_cvt_pk_bf16_f32 v102, v102, v104\nv_cvt_pk_bf16_f32 v103, v103, v105\nv_mfma_f32_32x32x4_2b_bf16 v[64:95], v[126:127], v[128:129], 0\nds_read_b32 v126, v131 offset:28800\n" \
  "s_cmp_lg_u32 s58, 0\ns_cbranch_scc1 9f\ns_waitcnt vmcnt(0)\nv_cmp_gt_u32 vcc, 32, v241\ns_nop 3\ns_cbranch_vccz 6f\n" \
  "global_load_dword v241, v240, %[wall] sc1\ns_branch 9f\n6:\nbuffer_inv sc1\ns_mov_b32 s58, 1\n9:\n" \
  "v_exp_f32 v96, v32\nv_exp_f32 v104, v33\nv_exp_f32 v97, v34\nv_exp_f32 v105, v35\nv_cvt_pk_bf16_f32 v96, v96, v104\nv_cvt_pk_bf16_f32 v97, v97, v105\n" \
  "v_exp_f32 v98, v36\nv_exp_f32 v104, v37\nv_exp_f32 v99, v38\nv_exp_f32 v105, v39\nv_cvt_pk_bf16_f32 v98, v98, v104\nv_cvt_pk_bf16_f32 v99, v99, v105\n" \
  "v_mfma_f32_32x32x16_bf16 v[16:31], v[114:117], v[100:103], v[16:31]\nds_read_b128 v[114:117], v132 offset:19824\nv_exp_f32 v100, v40\nv_exp_f32 v104, v41\nv_exp_f32 v101, v42\nv_exp_f32 v105, v43\n" \
  "v_cvt_pk_bf16_f32 v100, v100, v104\nv_cvt_pk_bf16_f32 v101, v101, v105\ns_waitcnt lgkmcnt(2)\nv_mfma_f32_32x32x16_bf16 v[0:15], v[110:113], v[96:99], v[0:15]\nv_exp_f32 v102, v44\nv_exp_f32 v104, v45\n" \
  "v_exp_f32 v103, v46\nv_exp_f32 v105, v47\nv_cvt_pk_bf16_f32 v102, v102, v104\nv_cvt_pk_bf16_f32 v103, v103, v105\nv_mfma_f32_32x32x16_bf16 v[64:79], v[106:109], v[118:121], v[64:79]\nv_exp_f32 v96, v48\n" \
  "v_exp_f32 v104, v49\nv_exp_f32 v97, v50\nv_exp_f32 v105, v51\nv_cvt_pk_bf16_f32 v96, v96, v104\nv_cvt_pk_bf16_f32 v97, v97, v105\ns_waitcnt lgkmcnt(0)\n" \
  "v_mfma_f32_32x32x16_bf16 v[0:15], v[114:117], v[100:103], v[0:15]\nv_exp_f32 v98, v52\nv_exp_f32 v104, v53\nv_exp_f32 v99, v54\nv_exp_f32 v105, v55\nv_cvt_pk_bf16_f32 v98, v98, v104\n" \
  "v_cvt_pk_bf16_f32 v99, v99, v105\nv_mfma_f32_32x32x16_bf16 v[80:95], v[106:109], v[122:125], v[80:95]\nds_read_b128 v[106:109], v130 offset:28800\nv_exp_f32 v100, v56\nv_exp_f32 v104, v57\nv_exp_f32 v101, v58\n" \
  "v_exp_f32 v105, v59\nv_cvt_pk_bf16_f32 v100, v100, v104\nv_cvt_pk_bf16_f32 v101, v101, v105\nv_mfma_f32_32x32x16_bf16 v[16:31], v[110:113], v[96:99], v[16:31]\nds_read_b128 v[110:113], v132 offset:24000\nv_exp_f32 v102, v60\n" \
  "v_exp_f32 v104, v61\nv_exp_f32 v103, v62\nv_exp_f32 v105, v63\nv_cvt_pk_bf16_f32 v102, v102, v104\nv_cvt_pk_bf16_f32 v103, v103, v105\nv_mfma_f32_32x32x4_2b_bf16 v[32:63], v[126:127], v[128:129], 0\n" \
  "ds_read_b32 v126, v131 offset:33600\nv_exp_f32 v96, v64\nv_exp_f32 v104, v65\nv_exp_f32 v97, v66\nv_exp_f32 v105, v67\nv_cvt_pk_bf16_f32 v96, v96, v104\n" \
  "v_cvt_pk_bf16_f32 v97, v97, v105\nv_exp_f32 v98, v68\nv_exp_f32 v104, v69\nv_exp_f32 v99, v70\nv_exp_f32 v105, v71\nv_cvt_pk_bf16_f32 v98, v98, v104\n" \
  "v_cvt_pk_bf16_f32 v99, v99, v105\nv_mfma_f32_32x32x16_bf16 v[16:31], v[114:117], v[100:103], v[16:31]\nds_read_b128 v[114:117], v132 offset:24624\nv_exp_f32 v100, v72\nv_exp_f32 v104, v73\nv_exp_f32 v101, v74\n" \
  "v_exp_f32 v105, v75\nv_cvt_pk_bf16_f32 v100, v100, v104\nv_cvt_pk_bf16_f32 v101, v101, v105\ns_waitcnt lgkmcnt(2)\nv_mfma_f32_32x32x16_bf16 v[0:15], v[110:113], v[96:99], v[0:15]\nv_exp_f32 v102, v76\n" \
  "v_exp_f32 v104, v77\nv_exp_f32 v103, v78\nv_exp_f32 v105, v79\nv_cvt_pk_bf16_f32 v102, v102, v104\nv_cvt_pk_bf16_f32 v103, v103, v105\nv_mfma_f32_32x32x16_bf16 v[32:47], v[106:109], v[118:121], v[32:47]\n" \
  "v_exp_f32 v96, v80\nv_exp_f32 v104, v81\nv_exp_f32 v97, v82\nv_exp_f32 v105, v83\nv_cvt_pk_bf16_f32 v96, v96, v104\nv_cvt_pk_bf16_f32 v97, v97, v105\n" \
  "s_waitcnt lgkmcnt(0)\nv_mfma_f32_32x32x16_bf16 v[0:15], v[114:117], v[100:103], v[0:15]\nv_exp_f32 v98, v84\nv_exp_f32 v104, v85\nv_exp_f32 v99, v86\nv_exp_f32 v105, v87\n" \
  "v_cvt_pk_bf16_f32 v98, v98, v104\nv_cvt_pk_bf16_f32 v99, v99, v105\nv_mfma_f32_32x32x16_bf16 v[48:63], v[106:109], v[122:125], v[48:63]\nds_read_b128 v[106:109], v130 offset:33600\nv_exp_f32 v100, v88\nv_exp_f32 v104, v89\n" \
  "v_exp_f32 v101, v90\nv_exp_f32 v105, v91\nv_cvt_pk_bf16_f32 v100, v100, v104\nv_cvt_pk_bf16_f32 v101, v101, v105\nv_mfma_f32_32x32x16_bf16 v[16:31], v[110:113], v[96:99], v[16:31]\nds_read_b128 v[110:113], v132 offset:28800\n" \
  "v_exp_f32 v102, v92\nv_exp_f32 v104, v93\nv_exp_f32 v103, v94\nv_exp_f32 v105, v95\nv_cvt_pk_bf16_f32 v102, v102, v104\nv_cvt_pk_bf16_f32 v103, v103, v105\n" \
  "v_mfma_f32_32x32x4_2b_bf16 v[64:95], v[126:127], v[128:129], 0\ns_cmp_lg_u32 s58, 0\ns_cbranch_scc1 9f\ns_waitcnt vmcnt(0)\nv_cmp_gt_u32 vcc, 32, v241\ns_nop 3\n" \
  "s_cbranch_vccz 6f\nglobal_load_dword v241, v240, %[wall] sc1\ns_branch 9f\n6:\nbuffer_inv sc1\ns_mov_b32 s58, 1\n" \
  "9:\nv_exp_f32 v96, v32\nv_exp_f32 v104, v33\nv_exp_f32 v97, v34\nv_exp_f32 v105, v35\nv_cvt_pk_bf16_f32 v96, v96, v104\n" \
  "v_cvt_pk_bf16_f32 v97, v97, v105\nv_exp_f32 v98, v36\nv_exp_f32 v104, v37\nv_exp_f32 v99, v38\nv_exp_f32 v105, v39\nv_cvt_pk_bf16_f32 v98, v98, v104\n" \
  "v_cvt_pk_bf16_f32 v99, v99, v105\nv_mfma_f32_32x32x16_bf16 v[16:31], v[114:117], v[100:103], v[16:31]\nds_read_b128 v[114:117], v132 offset:29424\nv_exp_f32 v100, v40\nv_exp_f32 v104, v41\nv_exp_f32 v101, v42\n" \
  "v_exp_f32 v105, v43\nv_cvt_pk_bf16_f32 v100, v100, v104\nv_cvt_pk_bf16_f32 v101, v101, v105\ns_waitcnt lgkmcnt(1)\nv_mfma_f32_32x32x16_bf16 v[0:15], v[110:113], v[96:99], v[0:15]\nv_exp_f32 v102, v44\n" \
  "v_exp_f32 v104, v45\nv_exp_f32 v103, v46\nv_exp_f32 v105, v47\nv_cvt_pk_bf16_f32 v102, v102, v104\nv_cvt_pk_bf16_f32 v103, v103, v105\nv_mfma_f32_32x32x16_bf16 v[64:79], v[106:109], v[118:121], v[64:79]\n" \
  "v_exp_f32 v96, v48\nv_exp_f32 v104, v49\nv_exp_f32 v97, v50\nv_exp_f32 v105, v51\nv_cvt_pk_bf16_f32 v96, v96, v104\nv_cvt_pk_bf16_f32 v97, v97, v105\n" \
  "s_waitcnt lgkmcnt(0)\nv_mfma_f32_32x32x16_bf16 v[0:15], v[114:117], v[100:103], v[0:15]\nv_exp_f32 v98, v52\nv_exp_f32 v104, v53\nv_exp_f32 v99, v54\nv_exp_f32 v105, v55\n" \
  "v_cvt_pk_bf16_f32 v98, v98, v104\nv_cvt_pk_bf16_f32 v99, v99, v105\nv_mfma_f32_32x32x16_bf16 v[80:95], v[106:109], v[122:125], v[80:95]\ns_setprio 0\nv_exp_f32 v100, v56\nv_exp_f32 v104, v57\n" \
  "v_exp_f32 v101, v58\nv_exp_f32 v105, v59\nv_cvt_pk_bf16_f32 v100, v100, v104\nv_cvt_pk_bf16_f32 v101, v101, v105\nv_mfma_f32_32x32x16_bf16 v[16:31], v[110:113], v[96:99], v[16:31]\nds_read_b128 v[110:113], v132 offset:33600\n" \
  "v_exp_f32 v102, v60\nv_exp_f32 v104, v61\nv_exp_f32 v103, v62\nv_exp_f32 v105, v63\nv_cvt_pk_bf16_f32 v102, v102, v104\nv_cvt_pk_bf16_f32 v103, v103, v105\n" \
  "v_exp_f32 v96, v64\nv_exp_f32 v104, v65\nv_exp_f32 v97, v66\nv_exp_f32 v105, v67\nv_cvt_pk_bf16_f32 v96, v96, v104\nv_cvt_pk_bf16_f32 v97, v97, v105\n" \
  "v_exp_f32 v98, v68\nv_exp_f32 v104, v69\nv_exp_f32 v99, v70\nv_exp_f32 v105, v71\nv_cvt_pk_bf16_f32 v98, v98, v104\nv_cvt_pk_bf16_f32 v99, v99, v105\n" \
  "v_mfma_f32_32x32x16_bf16 v[16:31], v[114:117], v[100:103], v[16:31]\nds_read_b128 v[114:117], v132 offset:34224\nv_exp_f32 v100, v72\nv_exp_f32 v104, v73\nv_exp_f32 v101, v74\nv_exp_f32 v105, v75\n" \
  "v_cvt_pk_bf16_f32 v100, v100, v104\nv_cvt_pk_bf16_f32 v101, v101, v105\ns_waitcnt lgkmcnt(1)\nv_mfma_f32_32x32x16_bf16 v[0:15], v[110:113], v[96:99], v[0:15]\nv_exp_f32 v102, v76\nv_exp_f32 v104, v77\n" \
  "v_exp_f32 v103, v78\nv_exp_f32 v105, v79\nv_cvt_pk_bf16_f32 v102, v102, v104\nv_cvt_pk_bf16_f32 v103, v103, v105\nv_exp_f32 v96, v80\nv_exp_f32 v104, v81\n" \
  "v_exp_f32 v97, v82\nv_exp_f32 v105, v83\nv_cvt_pk_bf16_f32 v96, v96, v104\nv_cvt_pk_bf16_f32 v97, v97, v105\ns_waitcnt lgkmcnt(0)\nv_mfma_f32_32x32x16_bf16 v[0:15], v[114:117], v[100:103], v[0:15]\n" \
  "v_exp_f32 v98, v84\nv_exp_f32 v104, v85\nv_exp_f32 v99, v86\nv_exp_f32 v105, v87\nv_cvt_pk_bf16_f32 v98, v98, v104\nv_cvt_pk_bf16_f32 v99, v99, v105\n" \
  "v_exp_f32 v100, v88\nv_exp_f32 v104, v89\nv_exp_f32 v101, v90\nv_exp_f32 v105, v91\nv_cvt_pk_bf16_f32 v100, v100, v104\nv_cvt_pk_bf16_f32 v101, v101, v105\n" \
  "v_mfma_f32_32x32x16_bf16 v[16:31], v[110:113], v[96:99], v[16:31]\nv_exp_f32 v102, v92\nv_exp_f32 v104, v93\nv_exp_f32 v103, v94\nv_exp_f32 v105, v95\nv_cvt_pk_bf16_f32 v102, v102, v104\n" \
  "v_cvt_pk_bf16_f32 v103, v103, v105\ns_cmp_lg_u32 s58, 0\ns_cbranch_scc1 9f\ns_mov_b32 s47, 0x200000\n5:\ns_waitcnt vmcnt(0)\n" \
  "v_cmp_gt_u32 vcc, 32, v241\ns_sub_u32 s47, s47, 1\ns_cbranch_scc1 6f\ns_nop 3\ns_cbranch_vccz 6f\ns_sleep 8\n" \
  "global_load_dword v241, v240, %[wall] sc1\ns_branch 5b\n6:\nbuffer_inv sc1\ns_mov_b32 s58, 1\n9:\n" \
  "s_waitcnt vmcnt(3)\ns_waitcnt lgkmcnt(0)\ns_nop 1\nv_mfma_f32_32x32x16_bf16 v[16:31], v[114:117], v[100:103], v[16:31]\ns_nop 7\ns_nop 7\n" \
  ""

typedef unsigned u32x32 __attribute__((ext_vector_type(32)));
typedef unsigned u32x8 __attribute__((ext_vector_type(8)));
typedef float f32x32 __attribute__((ext_vector_type(32)));
#define CLOB_208_243 "v208", "v209", "v210", "v211", "v212", "v213", "v214", "v215", "v216", "v217", "v218", "v219", "v220", "v221", "v222", "v223", "v224", "v225", "v226", "v227", "v228", "v229", "v230", "v231", "v232", "v233", "v234", "v235", "v236", "v237", "v238", "v239", "v240", "v241", "v242", "v243"
#define CAT16(a, b) __builtin_shufflevector(a, b, 0, 1, 2, 3, 4, 5, 6, 7, 8, 9, 10, 11, 12, 13, 14, 15, 16, 17, 18, 19, 20, 21, 22, 23, 24, 25, 26, 27, 28, 29, 30, 31)
DI float max3q(float a, float b, float c) { return __builtin_amdgcn_fmed3f(__builtin_amdgcn_fmed3f(a, b, __builtin_inff()), c, __builtin_inff()); }
DI float max16q(const f32x16& s) {
  return max3q(max3q(max3q(s[0], s[1], s[2]), max3q(s[3], s[4], s[5]), max3q(s[6], s[7], s[8])), max3q(max3q(s[9], s[10], s[11]), max3q(s[12], s[13], s[14]), s[15]), s[0]);
}
#define LO16(x) __builtin_shufflevector(x, x, 0, 1, 2, 3, 4, 5, 6, 7, 8, 9, 10, 11, 12, 13, 14, 15)
#define HI16(x) __builtin_shufflevector(x, x, 16, 17, 18, 19, 20, 21, 22, 23, 24, 25, 26, 27, 28, 29, 30, 31)
constexpr int FFN_PRE0 = 3, FFN_PRER = 6;
#define FFN_ASM \
  "v_mov_b32 v229, 1\nv_mov_b32 v230, 0\nbuffer_load_dwordx4 v[0:3], v251, %[rx], s44 offen offset:0 sc1\ns_add_u32 s49, s44, 16384\nbuffer_load_dwordx4 v[72:75], v251, %[rx], s49 offen offset:2048 sc1\nbuffer_load_dwordx4 v[4:7], v251, %[rx], s44 offen offset:1024 sc1\n" \
  "s_add_u32 s49, s44, 16384\nbuffer_load_dwordx4 v[76:79], v251, %[rx], s49 offen offset:3072 sc1\nbuffer_load_dwordx4 v[8:11], v251, %[rx], s44 offen offset:2048 sc1\ns_waitcnt vmcnt(3)\ns_barrier\nds_read_b128 v[208:211], v248 offset:32768\n" \
  "ds_read_b128 v[212:215], v248 offset:33792\nds_read_b128 v[216:219], v248 offset:34816\nds_read_b128 v[220:223], v248 offset:35840\ns_waitcnt lgkmcnt(3)\nv_mfma_f32_16x16x32_bf16 v[144:147], v[208:211], v[0:3], 0\nds_read_b128 v[224:227], v248 offset:36864\n" \
  "v_mfma_f32_16x16x32_bf16 v[148:151], v[208:211], v[72:75], 0\ns_add_u32 s49, s44, 20480\nbuffer_load_dwordx4 v[80:83], v251, %[rx], s49 offen offset:0 sc1\ns_waitcnt lgkmcnt(3)\nv_mfma_f32_16x16x32_bf16 v[152:155], v[212:215], v[0:3], 0\nds_read_b128 v[208:211], v248 offset:37888\n" \
  "v_mfma_f32_16x16x32_bf16 v[156:159], v[212:215], v[72:75], 0\ns_waitcnt lgkmcnt(3)\nv_mfma_f32_16x16x32_bf16 v[160:163], v[216:219], v[0:3], 0\nds_read_b128 v[212:215], v248 offset:38912\nv_mfma_f32_16x16x32_bf16 v[164:167], v[216:219], v[72:75], 0\nbuffer_load_dwordx4 v[12:15], v251, %[rx], s44 offen offset:3072 sc1\n" \
  "s_waitcnt lgkmcnt(3)\nv_mfma_f32_16x16x32_bf16 v[168:171], v[220:223], v[0:3], 0\nds_read_b128 v[216:219], v248 offset:39936\nv_mfma_f32_16x16x32_bf16 v[172:175], v[220:223], v[72:75], 0\ns_waitcnt lgkmcnt(3)\nv_mfma_f32_16x16x32_bf16 v[176:179], v[224:227], v[0:3], 0\n" \
  "ds_read_b128 v[220:223], v248 offset:40960\nv_mfma_f32_16x16x32_bf16 v[180:183], v[224:227], v[72:75], 0\ns_add_u32 s49, s44, 20480\nbuffer_load_dwordx4 v[84:87], v251, %[rx], s49 offen offset:1024 sc1\ns_waitcnt lgkmcnt(3)\nv_mfma_f32_16x16x32_bf16 v[184:187], v[208:211], v[0:3], 0\n" \
  "ds_read_b128 v[224:227], v248 offset:41984\nv_mfma_f32_16x16x32_bf16 v[188:191], v[208:211], v[72:75], 0\ns_waitcnt lgkmcnt(3)\nv_mfma_f32_16x16x32_bf16 v[192:195], v[212:215], v[0:3], 0\nds_read_b128 v[208:211], v248 offset:43008\nv_mfma_f32_16x16x32_bf16 v[196:199], v[212:215], v[72:75], 0\n" \
  "s_add_u32 s49, s44, 4096\nbuffer_load_dwordx4 v[16:19], v251, %[rx], s49 offen offset:0 sc1\ns_waitcnt lgkmcnt(3)\nv_mfma_f32_16x16x32_bf16 v[200:203], v[216:219], v[0:3], 0\nds_read_b128 v[212:215], v248 offset:44032\nv_mfma_f32_16x16x32_bf16 v[204:207], v[216:219], v[72:75], 0\n" \
  "s_waitcnt vmcnt(5)\ns_waitcnt lgkmcnt(3)\nv_mfma_f32_16x16x32_bf16 v[144:147], v[220:223], v[4:7], v[144:147]\nds_read_b128 v[216:219], v248 offset:45056\nv_mfma_f32_16x16x32_bf16 v[148:151], v[220:223], v[76:79], v[148:151]\ns_waitcnt lgkmcnt(3)\n" \
  "v_mfma_f32_16x16x32_bf16 v[152:155], v[224:227], v[4:7], v[152:155]\nds_read_b128 v[220:223], v248 offset:46080\nv_mfma_f32_16x16x32_bf16 v[156:159], v[224:227], v[76:79], v[156:159]\ns_add_u32 s49, s44, 20480\nbuffer_load_dwordx4 v[88:91], v251, %[rx], s49 offen offset:2048 sc1\ns_waitcnt lgkmcnt(3)\n" \
  "v_mfma_f32_16x16x32_bf16 v[160:163], v[208:211], v[4:7], v[160:163]\nds_read_b128 v[224:227], v248 offset:47104\nv_mfma_f32_16x16x32_bf16 v[164:167], v[208:211], v[76:79], v[164:167]\ns_waitcnt lgkmcnt(3)\nv_mfma_f32_16x16x32_bf16 v[168:171], v[212:215], v[4:7], v[168:171]\nds_read_b128 v[208:211], v248 offset:48128\n" \
  "v_mfma_f32_16x16x32_bf16 v[172:175], v[212:215], v[76:79], v[172:175]\ns_add_u32 m0, s40, 81920\ns_add_u32 s49, s41, 49152\nbuffer_load_dwordx4 v251, %[rw], s49 offen sc1 lds\ns_waitcnt lgkmcnt(3)\nv_mfma_f32_16x16x32_bf16 v[176:179], v[216:219], v[4:7], v[176:179]\n" \
  "ds_read_b128 v[212:215], v248 offset:49152\nv_mfma_f32_16x16x32_bf16 v[180:183], v[216:219], v[76:79], v[180:183]\ns_waitcnt lgkmcnt(3)\nv_mfma_f32_16x16x32_bf16 v[184:187], v[220:223], v[4:7], v[184:187]\nds_read_b128 v[216:219], v248 offset:50176\nv_mfma_f32_16x16x32_bf16 v[188:191], v[220:223], v[76:79], v[188:191]\n" \
  "s_add_u32 s49, s44, 4096\nbuffer_load_dwordx4 v[20:23], v251, %[rx], s49 offen offset:1024 sc1\ns_waitcnt lgkmcnt(3)\nv_mfma_f32_16x16x32_bf16 v[192:195], v[224:227], v[4:7], v[192:195]\nds_read_b128 v[220:223], v248 offset:51200\nv_mfma_f32_16x16x32_bf16 v[196:199], v[224:227], v[76:79], v[196:199]\n" \
  "s_waitcnt lgkmcnt(3)\nv_mfma_f32_16x16x32_bf16 v[200:203], v[208:211], v[4:7], v[200:203]\nds_read_b128 v[224:227], v248 offset:52224\nv_mfma_f32_16x16x32_bf16 v[204:207], v[208:211], v[76:79], v[204:207]\ns_add_u32 s49, s44, 20480\nbuffer_load_dwordx4 v[92:95], v251, %[rx], s49 offen offset:3072 sc1\n" \
  "s_waitcnt vmcnt(7)\ns_barrier\ns_waitcnt lgkmcnt(3)\nv_mfma_f32_16x16x32_bf16 v[144:147], v[212:215], v[8:11], v[144:147]\nds_read_b128 v[208:211], v248 offset:53248\nv_mfma_f32_16x16x32_bf16 v[148:151], v[212:215], v[80:83], v[148:151]\n" \
  "s_waitcnt lgkmcnt(3)\nv_mfma_f32_16x16x32_bf16 v[152:155], v[216:219], v[8:11], v[152:155]\nds_read_b128 v[212:215], v248 offset:54272\nv_mfma_f32_16x16x32_bf16 v[156:159], v[216:219], v[80:83], v[156:159]\ns_waitcnt lgkmcnt(3)\nv_mfma_f32_16x16x32_bf16 v[160:163], v[220:223], v[8:11], v[160:163]\n" \
  "ds_read_b128 v[216:219], v249 offset:0\nv_mfma_f32_16x16x32_bf16 v[164:167], v[220:223], v[80:83], v[164:167]\ns_add_u32 m0, s40, 90112\ns_add_u32 s49, s41, 57344\nbuffer_load_dwordx4 v251, %[rw], s49 offen sc1 lds\ns_waitcnt lgkmcnt(3)\n" \
  "v_mfma_f32_16x16x32_bf16 v[168:171], v[224:227], v[8:11], v[168:171]\nds_read_b128 v[220:223], v249 offset:1024\nv_mfma_f32_16x16x32_bf16 v[172:175], v[224:227], v[80:83], v[172:175]\ns_waitcnt lgkmcnt(3)\nv_mfma_f32_16x16x32_bf16 v[176:179], v[208:211], v[8:11], v[176:179]\nds_read_b128 v[224:227], v249 offset:2048\n" \
  "v_mfma_f32_16x16x32_bf16 v[180:183], v[208:211], v[80:83], v[180:183]\ns_add_u32 m0, s40, 98304\ns_add_u32 s49, s41, 65536\nbuffer_load_dwordx4 v251, %[rw], s49 offen sc1 lds\ns_waitcnt lgkmcnt(3)\nv_mfma_f32_16x16x32_bf16 v[184:187], v[212:215], v[8:11], v[184:187]\n" \
  "ds_read_b128 v[208:211], v249 offset:3072\nv_mfma_f32_16x16x32_bf16 v[188:191], v[212:215], v[80:83], v[188:191]\ns_waitcnt lgkmcnt(3)\nv_mfma_f32_16x16x32_bf16 v[192:195], v[216:219], v[8:11], v[192:195]\nds_read_b128 v[212:215], v249 offset:4096\nv_mfma_f32_16x16x32_bf16 v[196:199], v[216:219], v[80:83], v[196:199]\n" \
  "s_add_u32 s49, s44, 4096\nbuffer_load_dwordx4 v[24:27], v251, %[rx], s49 offen offset:2048 sc1\ns_waitcnt lgkmcnt(3)\nv_mfma_f32_16x16x32_bf16 v[200:203], v[220:223], v[8:11], v[200:203]\nds_read_b128 v[216:219], v249 offset:5120\nv_mfma_f32_16x16x32_bf16 v[204:207], v[220:223], v[80:83], v[204:207]\n" \
  "s_waitcnt vmcnt(8)\ns_waitcnt lgkmcnt(3)\nv_mfma_f32_16x16x32_bf16 v[144:147], v[224:227], v[12:15], v[144:147]\nds_read_b128 v[220:223], v249 offset:6144\nv_mfma_f32_16x16x32_bf16 v[148:151], v[224:227], v[84:87], v[148:151]\ns_add_u32 s49, s44, 24576\n" \
  "buffer_load_dwordx4 v[96:99], v251, %[rx], s49 offen offset:0 sc1\ns_waitcnt lgkmcnt(3)\nv_mfma_f32_16x16x32_bf16 v[152:155], v[208:211], v[12:15], v[152:155]\nds_read_b128 v[224:227], v249 offset:7168\nv_mfma_f32_16x16x32_bf16 v[156:159], v[208:211], v[84:87], v[156:159]\ns_waitcnt lgkmcnt(3)\n" \
  "v_mfma_f32_16x16x32_bf16 v[160:163], v[212:215], v[12:15], v[160:163]\nds_read_b128 v[208:211], v249 offset:8192\nv_mfma_f32_16x16x32_bf16 v[164:167], v[212:215], v[84:87], v[164:167]\ns_waitcnt lgkmcnt(3)\nv_mfma_f32_16x16x32_bf16 v[168:171], v[216:219], v[12:15], v[168:171]\nds_read_b128 v[212:215], v249 offset:9216\n" \
  "v_mfma_f32_16x16x32_bf16 v[172:175], v[216:219], v[84:87], v[172:175]\ns_add_u32 s49, s44, 4096\nbuffer_load_dwordx4 v[28:31], v251, %[rx], s49 offen offset:3072 sc1\ns_waitcnt lgkmcnt(3)\nv_mfma_f32_16x16x32_bf16 v[176:179], v[220:223], v[12:15], v[176:179]\nds_read_b128 v[216:219], v249 offset:10240\n" \
  "v_mfma_f32_16x16x32_bf16 v[180:183], v[220:223], v[84:87], v[180:183]\ns_waitcnt lgkmcnt(3)\nv_mfma_f32_16x16x32_bf16 v[184:187], v[224:227], v[12:15], v[184:187]\nds_read_b128 v[220:223], v249 offset:11264\nv_mfma_f32_16x16x32_bf16 v[188:191], v[224:227], v[84:87], v[188:191]\ns_add_u32 s49, s44, 24576\n" \
  "buffer_load_dwordx4 v[100:103], v251, %[rx], s49 offen offset:1024 sc1\ns_waitcnt lgkmcnt(3)\nv_mfma_f32_16x16x32_bf16 v[192:195], v[208:211], v[12:15], v[192:195]\nds_read_b128 v[224:227], v249 offset:12288\nv_mfma_f32_16x16x32_bf16 v[196:199], v[208:211], v[84:87], v[196:199]\ns_waitcnt lgkmcnt(3)\n" \
  "v_mfma_f32_16x16x32_bf16 v[200:203], v[212:215], v[12:15], v[200:203]\nds_read_b128 v[208:211], v249 offset:13312\nv_mfma_f32_16x16x32_bf16 v[204:207], v[212:215], v[84:87], v[204:207]\ns_add_u32 m0, s40, 106496\ns_add_u32 s49, s41, 73728\nbuffer_load_dwordx4 v251, %[rw], s49 offen sc1 lds\n" \
  "s_waitcnt vmcnt(10)\ns_waitcnt lgkmcnt(3)\nv_mfma_f32_16x16x32_bf16 v[144:147], v[216:219], v[16:19], v[144:147]\nds_read_b128 v[212:215], v249 offset:14336\nv_mfma_f32_16x16x32_bf16 v[148:151], v[216:219], v[88:91], v[148:151]\ns_waitcnt lgkmcnt(3)\n" \
  "v_mfma_f32_16x16x32_bf16 v[152:155], v[220:223], v[16:19], v[152:155]\nds_read_b128 v[216:219], v249 offset:15360\nv_mfma_f32_16x16x32_bf16 v[156:159], v[220:223], v[88:91], v[156:159]\ns_add_u32 m0, s40, 114688\ns_add_u32 s49, s41, 81920\nbuffer_load_dwordx4 v251, %[rw], s49 offen sc1 lds\n" \
  "s_waitcnt lgkmcnt(3)\nv_mfma_f32_16x16x32_bf16 v[160:163], v[224:227], v[16:19], v[160:163]\nds_read_b128 v[220:223], v249 offset:16384\nv_mfma_f32_16x16x32_bf16 v[164:167], v[224:227], v[88:91], v[164:167]\ns_waitcnt lgkmcnt(3)\nv_mfma_f32_16x16x32_bf16 v[168:171], v[208:211], v[16:19], v[168:171]\n" \
  "ds_read_b128 v[224:227], v249 offset:17408\nv_mfma_f32_16x16x32_bf16 v[172:175], v[208:211], v[88:91], v[172:175]\ns_waitcnt vmcnt(10)\ns_barrier\ns_waitcnt lgkmcnt(3)\nv_mfma_f32_16x16x32_bf16 v[176:179], v[212:215], v[16:19], v[176:179]\n" \
  "ds_read_b128 v[208:211], v249 offset:18432\nv_mfma_f32_16x16x32_bf16 v[180:183], v[212:215], v[88:91], v[180:183]\ns_add_u32 m0, s40, 122880\ns_add_u32 s49, s41, 90112\nbuffer_load_dwordx4 v251, %[rw], s49 offen sc1 lds\ns_waitcnt lgkmcnt(3)\n" \
  "v_mfma_f32_16x16x32_bf16 v[184:187], v[216:219], v[16:19], v[184:187]\nds_read_b128 v[212:215], v249 offset:19456\nv_mfma_f32_16x16x32_bf16 v[188:191], v[216:219], v[88:91], v[188:191]\ns_waitcnt lgkmcnt(3)\nv_mfma_f32_16x16x32_bf16 v[192:195], v[220:223], v[16:19], v[192:195]\nds_read_b128 v[216:219], v249 offset:20480\n" \
  "v_mfma_f32_16x16x32_bf16 v[196:199], v[220:223], v[88:91], v[196:199]\ns_add_u32 s49, s44, 8192\nbuffer_load_dwordx4 v[32:35], v251, %[rx], s49 offen offset:0 sc1\ns_waitcnt lgkmcnt(3)\nv_mfma_f32_16x16x32_bf16 v[200:203], v[224:227], v[16:19], v[200:203]\nds_read_b128 v[220:223], v249 offset:21504\n" \
  "v_mfma_f32_16x16x32_bf16 v[204:207], v[224:227], v[88:91], v[204:207]\ns_waitcnt vmcnt(10)\ns_waitcnt lgkmcnt(3)\nv_mfma_f32_16x16x32_bf16 v[144:147], v[208:211], v[20:23], v[144:147]\nds_read_b128 v[224:227], v249 offset:22528\nv_mfma_f32_16x16x32_bf16 v[148:151], v[208:211], v[92:95], v[148:151]\n" \
  "s_add_u32 s49, s44, 24576\nbuffer_load_dwordx4 v[104:107], v251, %[rx], s49 offen offset:2048 sc1\ns_waitcnt lgkmcnt(3)\nv_mfma_f32_16x16x32_bf16 v[152:155], v[212:215], v[20:23], v[152:155]\nds_read_b128 v[208:211], v249 offset:23552\nv_mfma_f32_16x16x32_bf16 v[156:159], v[212:215], v[92:95], v[156:159]\n" \
  "s_waitcnt lgkmcnt(3)\nv_mfma_f32_16x16x32_bf16 v[160:163], v[216:219], v[20:23], v[160:163]\nds_read_b128 v[212:215], v249 offset:24576\nv_mfma_f32_16x16x32_bf16 v[164:167], v[216:219], v[92:95], v[164:167]\ns_add_u32 s49, s44, 8192\nbuffer_load_dwordx4 v[36:39], v251, %[rx], s49 offen offset:1024 sc1\n" \
  "s_waitcnt lgkmcnt(3)\nv_mfma_f32_16x16x32_bf16 v[168:171], v[220:223], v[20:23], v[168:171]\nds_read_b128 v[216:219], v249 offset:25600\nv_mfma_f32_16x16x32_bf16 v[172:175], v[220:223], v[92:95], v[172:175]\ns_waitcnt lgkmcnt(3)\nv_mfma_f32_16x16x32_bf16 v[176:179], v[224:227], v[20:23], v[176:179]\n" \
  "ds_read_b128 v[220:223], v249 offset:26624\nv_mfma_f32_16x16x32_bf16 v[180:183], v[224:227], v[92:95], v[180:183]\ns_waitcnt lgkmcnt(3)\nv_mfma_f32_16x16x32_bf16 v[184:187], v[208:211], v[20:23], v[184:187]\nds_read_b128 v[224:227], v249 offset:27648\nv_mfma_f32_16x16x32_bf16 v[188:191], v[208:211], v[92:95], v[188:191]\n" \
  "s_add_u32 s49, s44, 24576\nbuffer_load_dwordx4 v[108:111], v251, %[rx], s49 offen offset:3072 sc1\ns_waitcnt lgkmcnt(3)\nv_mfma_f32_16x16x32_bf16 v[192:195], v[212:215], v[20:23], v[192:195]\nds_read_b128 v[208:211], v249 offset:28672\nv_mfma_f32_16x16x32_bf16 v[196:199], v[212:215], v[92:95], v[196:199]\n" \
  "s_waitcnt lgkmcnt(3)\nv_mfma_f32_16x16x32_bf16 v[200:203], v[216:219], v[20:23], v[200:203]\nds_read_b128 v[212:215], v249 offset:29696\nv_mfma_f32_16x16x32_bf16 v[204:207], v[216:219], v[92:95], v[204:207]\ns_add_u32 s49, s44, 8192\nbuffer_load_dwordx4 v[40:43], v251, %[rx], s49 offen offset:2048 sc1\n" \
  "s_waitcnt vmcnt(10)\ns_barrier\ns_waitcnt lgkmcnt(3)\nv_mfma_f32_16x16x32_bf16 v[144:147], v[220:223], v[24:27], v[144:147]\nds_read_b128 v[216:219], v249 offset:30720\nv_mfma_f32_16x16x32_bf16 v[148:151], v[220:223], v[96:99], v[148:151]\n" \
  "s_waitcnt lgkmcnt(3)\nv_mfma_f32_16x16x32_bf16 v[152:155], v[224:227], v[24:27], v[152:155]\nds_read_b128 v[220:223], v249 offset:31744\nv_mfma_f32_16x16x32_bf16 v[156:159], v[224:227], v[96:99], v[156:159]\ns_add_u32 s49, s44, 28672\nbuffer_load_dwordx4 v[112:115], v251, %[rx], s49 offen offset:0 sc1\n" \
  "s_waitcnt lgkmcnt(3)\nv_mfma_f32_16x16x32_bf16 v[160:163], v[208:211], v[24:27], v[160:163]\nds_read_b128 v[224:227], v249 offset:32768\nv_mfma_f32_16x16x32_bf16 v[164:167], v[208:211], v[96:99], v[164:167]\ns_waitcnt lgkmcnt(3)\nv_mfma_f32_16x16x32_bf16 v[168:171], v[212:215], v[24:27], v[168:171]\n" \
  "ds_read_b128 v[208:211], v249 offset:33792\nv_mfma_f32_16x16x32_bf16 v[172:175], v[212:215], v[96:99], v[172:175]\ns_add_u32 m0, s40, 131072\ns_add_u32 s49, s41, 98304\nbuffer_load_dwordx4 v251, %[rw], s49 offen sc1 lds\ns_waitcnt lgkmcnt(3)\n" \
  "v_mfma_f32_16x16x32_bf16 v[176:179], v[216:219], v[24:27], v[176:179]\nds_read_b128 v[212:215], v249 offset:34816\nv_mfma_f32_16x16x32_bf16 v[180:183], v[216:219], v[96:99], v[180:183]\ns_waitcnt lgkmcnt(3)\nv_mfma_f32_16x16x32_bf16 v[184:187], v[220:223], v[24:27], v[184:187]\nds_read_b128 v[216:219], v249 offset:35840\n" \
  "v_mfma_f32_16x16x32_bf16 v[188:191], v[220:223], v[96:99], v[188:191]\ns_waitcnt lgkmcnt(3)\nv_mfma_f32_16x16x32_bf16 v[192:195], v[224:227], v[24:27], v[192:195]\nds_read_b128 v[220:223], v249 offset:36864\nv_mfma_f32_16x16x32_bf16 v[196:199], v[224:227], v[96:99], v[196:199]\ns_add_u32 m0, s40, 139264\n" \
  "s_add_u32 s49, s41, 106496\nbuffer_load_dwordx4 v251, %[rw], s49 offen sc1 lds\ns_waitcnt lgkmcnt(3)\nv_mfma_f32_16x16x32_bf16 v[200:203], v[208:211], v[24:27], v[200:203]\nds_read_b128 v[224:227], v249 offset:37888\nv_mfma_f32_16x16x32_bf16 v[204:207], v[208:211], v[96:99], v[204:207]\n" \
  "s_waitcnt vmcnt(11)\ns_waitcnt lgkmcnt(3)\nv_mfma_f32_16x16x32_bf16 v[144:147], v[212:215], v[28:31], v[144:147]\nds_read_b128 v[208:211], v249 offset:38912\nv_mfma_f32_16x16x32_bf16 v[148:151], v[212:215], v[100:103], v[148:151]\ns_add_u32 m0, s40, 0\n" \
  "s_add_u32 s49, s41, 114688\nbuffer_load_dwordx4 v251, %[rw], s49 offen sc1 lds\ns_waitcnt lgkmcnt(3)\nv_mfma_f32_16x16x32_bf16 v[152:155], v[216:219], v[28:31], v[152:155]\nds_read_b128 v[212:215], v249 offset:39936\nv_mfma_f32_16x16x32_bf16 v[156:159], v[216:219], v[100:103], v[156:159]\n" \
  "s_waitcnt lgkmcnt(3)\nv_mfma_f32_16x16x32_bf16 v[160:163], v[220:223], v[28:31], v[160:163]\nds_read_b128 v[216:219], v249 offset:40960\nv_mfma_f32_16x16x32_bf16 v[164:167], v[220:223], v[100:103], v[164:167]\ns_add_u32 s49, s44, 8192\nbuffer_load_dwordx4 v[44:47], v251, %[rx], s49 offen offset:3072 sc1\n" \
  "s_waitcnt lgkmcnt(3)\nv_mfma_f32_16x16x32_bf16 v[168:171], v[224:227], v[28:31], v[168:171]\nds_read_b128 v[220:223], v249 offset:41984\nv_mfma_f32_16x16x32_bf16 v[172:175], v[224:227], v[100:103], v[172:175]\ns_waitcnt lgkmcnt(3)\nv_mfma_f32_16x16x32_bf16 v[176:179], v[208:211], v[28:31], v[176:179]\n" \
  "ds_read_b128 v[224:227], v249 offset:43008\nv_mfma_f32_16x16x32_bf16 v[180:183], v[208:211], v[100:103], v[180:183]\ns_add_u32 s49, s44, 28672\nbuffer_load_dwordx4 v[116:119], v251, %[rx], s49 offen offset:1024 sc1\ns_waitcnt lgkmcnt(3)\nv_mfma_f32_16x16x32_bf16 v[184:187], v[212:215], v[28:31], v[184:187]\n" \
  "ds_read_b128 v[208:211], v249 offset:44032\nv_mfma_f32_16x16x32_bf16 v[188:191], v[212:215], v[100:103], v[188:191]\ns_waitcnt lgkmcnt(3)\nv_mfma_f32_16x16x32_bf16 v[192:195], v[216:219], v[28:31], v[192:195]\nds_read_b128 v[212:215], v249 offset:45056\nv_mfma_f32_16x16x32_bf16 v[196:199], v[216:219], v[100:103], v[196:199]\n" \
  "s_waitcnt lgkmcnt(3)\nv_mfma_f32_16x16x32_bf16 v[200:203], v[220:223], v[28:31], v[200:203]\nds_read_b128 v[216:219], v249 offset:46080\nv_mfma_f32_16x16x32_bf16 v[204:207], v[220:223], v[100:103], v[204:207]\ns_add_u32 s49, s44, 12288\nbuffer_load_dwordx4 v[48:51], v251, %[rx], s49 offen offset:0 sc1\n" \
  "s_waitcnt vmcnt(10)\ns_barrier\ns_waitcnt lgkmcnt(3)\nv_mfma_f32_16x16x32_bf16 v[144:147], v[224:227], v[32:35], v[144:147]\nds_read_b128 v[220:223], v249 offset:47104\nv_mfma_f32_16x16x32_bf16 v[148:151], v[224:227], v[104:107], v[148:151]\n" \
  "s_waitcnt lgkmcnt(3)\nv_mfma_f32_16x16x32_bf16 v[152:155], v[208:211], v[32:35], v[152:155]\nds_read_b128 v[224:227], v249 offset:48128\nv_mfma_f32_16x16x32_bf16 v[156:159], v[208:211], v[104:107], v[156:159]\ns_add_u32 s49, s44, 28672\nbuffer_load_dwordx4 v[120:123], v251, %[rx], s49 offen offset:2048 sc1\n" \
  "s_waitcnt lgkmcnt(3)\nv_mfma_f32_16x16x32_bf16 v[160:163], v[212:215], v[32:35], v[160:163]\nds_read_b128 v[208:211], v249 offset:49152\nv_mfma_f32_16x16x32_bf16 v[164:167], v[212:215], v[104:107], v[164:167]\ns_waitcnt lgkmcnt(3)\nv_mfma_f32_16x16x32_bf16 v[168:171], v[216:219], v[32:35], v[168:171]\n" \
  "ds_read_b128 v[212:215], v249 offset:50176\nv_mfma_f32_16x16x32_bf16 v[172:175], v[216:219], v[104:107], v[172:175]\ns_add_u32 s49, s44, 12288\nbuffer_load_dwordx4 v[52:55], v251, %[rx], s49 offen offset:1024 sc1\ns_waitcnt lgkmcnt(3)\nv_mfma_f32_16x16x32_bf16 v[176:179], v[220:223], v[32:35], v[176:179]\n" \
  "ds_read_b128 v[216:219], v249 offset:51200\nv_mfma_f32_16x16x32_bf16 v[180:183], v[220:223], v[104:107], v[180:183]\ns_waitcnt lgkmcnt(3)\nv_mfma_f32_16x16x32_bf16 v[184:187], v[224:227], v[32:35], v[184:187]\nds_read_b128 v[220:223], v249 offset:52224\nv_mfma_f32_16x16x32_bf16 v[188:191], v[224:227], v[104:107], v[188:191]\n" \
  "s_add_u32 s49, s44, 28672\nbuffer_load_dwordx4 v[124:127], v251, %[rx], s49 offen offset:3072 sc1\ns_waitcnt lgkmcnt(3)\nv_mfma_f32_16x16x32_bf16 v[192:195], v[208:211], v[32:35], v[192:195]\nds_read_b128 v[224:227], v249 offset:53248\nv_mfma_f32_16x16x32_bf16 v[196:199], v[208:211], v[104:107], v[196:199]\n" \
  "s_waitcnt lgkmcnt(3)\nv_mfma_f32_16x16x32_bf16 v[200:203], v[212:215], v[32:35], v[200:203]\nds_read_b128 v[208:211], v249 offset:54272\nv_mfma_f32_16x16x32_bf16 v[204:207], v[212:215], v[104:107], v[204:207]\ns_waitcnt vmcnt(11)\ns_waitcnt lgkmcnt(3)\n" \
  "v_mfma_f32_16x16x32_bf16 v[144:147], v[216:219], v[36:39], v[144:147]\nds_read_b128 v[212:215], v250 offset:0\nv_mfma_f32_16x16x32_bf16 v[148:151], v[216:219], v[108:111], v[148:151]\ns_add_u32 m0, s40, 8192\ns_add_u32 s49, s41, 122880\nbuffer_load_dwordx4 v251, %[rw], s49 offen sc1 lds\n" \
  "s_waitcnt lgkmcnt(3)\nv_mfma_f32_16x16x32_bf16 v[152:155], v[220:223], v[36:39], v[152:155]\nds_read_b128 v[216:219], v250 offset:1024\nv_mfma_f32_16x16x32_bf16 v[156:159], v[220:223], v[108:111], v[156:159]\ns_waitcnt lgkmcnt(3)\nv_mfma_f32_16x16x32_bf16 v[160:163], v[224:227], v[36:39], v[160:163]\n" \
  "ds_read_b128 v[220:223], v250 offset:2048\nv_mfma_f32_16x16x32_bf16 v[164:167], v[224:227], v[108:111], v[164:167]\ns_add_u32 m0, s40, 16384\ns_add_u32 s49, s41, 131072\nbuffer_load_dwordx4 v251, %[rw], s49 offen sc1 lds\ns_waitcnt lgkmcnt(3)\n" \
  "v_mfma_f32_16x16x32_bf16 v[168:171], v[208:211], v[36:39], v[168:171]\nds_read_b128 v[224:227], v250 offset:3072\nv_mfma_f32_16x16x32_bf16 v[172:175], v[208:211], v[108:111], v[172:175]\ns_waitcnt lgkmcnt(3)\nv_mfma_f32_16x16x32_bf16 v[176:179], v[212:215], v[36:39], v[176:179]\nds_read_b128 v[208:211], v250 offset:4096\n" \
  "v_mfma_f32_16x16x32_bf16 v[180:183], v[212:215], v[108:111], v[180:183]\ns_add_u32 m0, s40, 24576\ns_add_u32 s49, s41, 139264\nbuffer_load_dwordx4 v251, %[rw], s49 offen sc1 lds\ns_waitcnt lgkmcnt(3)\nv_mfma_f32_16x16x32_bf16 v[184:187], v[216:219], v[36:39], v[184:187]\n" \
  "ds_read_b128 v[212:215], v250 offset:5120\nv_mfma_f32_16x16x32_bf16 v[188:191], v[216:219], v[108:111], v[188:191]\ns_waitcnt lgkmcnt(3)\nv_mfma_f32_16x16x32_bf16 v[192:195], v[220:223], v[36:39], v[192:195]\nds_read_b128 v[216:219], v250 offset:6144\nv_mfma_f32_16x16x32_bf16 v[196:199], v[220:223], v[108:111], v[196:199]\n" \
  "s_add_u32 m0, s40, 32768\ns_add_u32 s49, s41, 147456\nbuffer_load_dwordx4 v251, %[rw], s49 offen sc1 lds\ns_waitcnt lgkmcnt(3)\nv_mfma_f32_16x16x32_bf16 v[200:203], v[224:227], v[36:39], v[200:203]\nds_read_b128 v[220:223], v250 offset:7168\n" \
  "v_mfma_f32_16x16x32_bf16 v[204:207], v[224:227], v[108:111], v[204:207]\ns_waitcnt vmcnt(13)\ns_waitcnt lgkmcnt(3)\nv_mfma_f32_16x16x32_bf16 v[144:147], v[208:211], v[40:43], v[144:147]\nds_read_b128 v[224:227], v250 offset:8192\nv_mfma_f32_16x16x32_bf16 v[148:151], v[208:211], v[112:115], v[148:151]\n" \
  "s_waitcnt lgkmcnt(3)\nv_mfma_f32_16x16x32_bf16 v[152:155], v[212:215], v[40:43], v[152:155]\nds_read_b128 v[208:211], v250 offset:9216\nv_mfma_f32_16x16x32_bf16 v[156:159], v[212:215], v[112:115], v[156:159]\ns_add_u32 s49, s44, 12288\nbuffer_load_dwordx4 v[56:59], v251, %[rx], s49 offen offset:2048 sc1\n" \
  "s_waitcnt lgkmcnt(3)\nv_mfma_f32_16x16x32_bf16 v[160:163], v[216:219], v[40:43], v[160:163]\nds_read_b128 v[212:215], v250 offset:10240\nv_mfma_f32_16x16x32_bf16 v[164:167], v[216:219], v[112:115], v[164:167]\ns_waitcnt lgkmcnt(3)\nv_mfma_f32_16x16x32_bf16 v[168:171], v[220:223], v[40:43], v[168:171]\n" \
  "ds_read_b128 v[216:219], v250 offset:11264\nv_mfma_f32_16x16x32_bf16 v[172:175], v[220:223], v[112:115], v[172:175]\ns_add_u32 s49, s44, 32768\nbuffer_load_dwordx4 v[128:131], v251, %[rx], s49 offen offset:0 sc1\ns_waitcnt vmcnt(12)\ns_barrier\n" \
  "s_waitcnt lgkmcnt(3)\nv_mfma_f32_16x16x32_bf16 v[176:179], v[224:227], v[40:43], v[176:179]\nds_read_b128 v[220:223], v250 offset:12288\nv_mfma_f32_16x16x32_bf16 v[180:183], v[224:227], v[112:115], v[180:183]\ns_waitcnt lgkmcnt(3)\nv_mfma_f32_16x16x32_bf16 v[184:187], v[208:211], v[40:43], v[184:187]\n" \
  "ds_read_b128 v[224:227], v250 offset:13312\nv_mfma_f32_16x16x32_bf16 v[188:191], v[208:211], v[112:115], v[188:191]\ns_add_u32 s49, s44, 12288\nbuffer_load_dwordx4 v[60:63], v251, %[rx], s49 offen offset:3072 sc1\ns_waitcnt lgkmcnt(3)\nv_mfma_f32_16x16x32_bf16 v[192:195], v[212:215], v[40:43], v[192:195]\n" \
  "ds_read_b128 v[208:211], v250 offset:14336\nv_mfma_f32_16x16x32_bf16 v[196:199], v[212:215], v[112:115], v[196:199]\ns_waitcnt lgkmcnt(3)\nv_mfma_f32_16x16x32_bf16 v[200:203], v[216:219], v[40:43], v[200:203]\nds_read_b128 v[212:215], v250 offset:15360\nv_mfma_f32_16x16x32_bf16 v[204:207], v[216:219], v[112:115], v[204:207]\n" \
  "s_add_u32 s49, s44, 32768\nbuffer_load_dwordx4 v[132:135], v251, %[rx], s49 offen offset:1024 sc1\ns_waitcnt vmcnt(12)\ns_waitcnt lgkmcnt(3)\nv_mfma_f32_16x16x32_bf16 v[144:147], v[220:223], v[44:47], v[144:147]\nds_read_b128 v[216:219], v250 offset:16384\n" \
  "v_mfma_f32_16x16x32_bf16 v[148:151], v[220:223], v[116:119], v[148:151]\ns_waitcnt lgkmcnt(3)\nv_mfma_f32_16x16x32_bf16 v[152:155], v[224:227], v[44:47], v[152:155]\nds_read_b128 v[220:223], v250 offset:17408\nv_mfma_f32_16x16x32_bf16 v[156:159], v[224:227], v[116:119], v[156:159]\ns_waitcnt lgkmcnt(3)\n" \
  "v_mfma_f32_16x16x32_bf16 v[160:163], v[208:211], v[44:47], v[160:163]\nds_read_b128 v[224:227], v250 offset:18432\nv_mfma_f32_16x16x32_bf16 v[164:167], v[208:211], v[116:119], v[164:167]\ns_add_u32 s49, s44, 16384\nbuffer_load_dwordx4 v[64:67], v251, %[rx], s49 offen offset:0 sc1\ns_waitcnt lgkmcnt(3)\n" \
  "v_mfma_f32_16x16x32_bf16 v[168:171], v[212:215], v[44:47], v[168:171]\nds_read_b128 v[208:211], v250 offset:19456\nv_mfma_f32_16x16x32_bf16 v[172:175], v[212:215], v[116:119], v[172:175]\ns_waitcnt lgkmcnt(3)\nv_mfma_f32_16x16x32_bf16 v[176:179], v[216:219], v[44:47], v[176:179]\nds_read_b128 v[212:215], v250 offset:20480\n" \
  "v_mfma_f32_16x16x32_bf16 v[180:183], v[216:219], v[116:119], v[180:183]\ns_add_u32 s49, s44, 32768\nbuffer_load_dwordx4 v[136:139], v251, %[rx], s49 offen offset:2048 sc1\ns_waitcnt lgkmcnt(3)\nv_mfma_f32_16x16x32_bf16 v[184:187], v[220:223], v[44:47], v[184:187]\nds_read_b128 v[216:219], v250 offset:21504\n" \
  "v_mfma_f32_16x16x32_bf16 v[188:191], v[220:223], v[116:119], v[188:191]\ns_waitcnt lgkmcnt(3)\nv_mfma_f32_16x16x32_bf16 v[192:195], v[224:227], v[44:47], v[192:195]\nds_read_b128 v[220:223], v250 offset:22528\nv_mfma_f32_16x16x32_bf16 v[196:199], v[224:227], v[116:119], v[196:199]\ns_add_u32 s49, s44, 16384\n" \
  "buffer_load_dwordx4 v[68:71], v251, %[rx], s49 offen offset:1024 sc1\ns_waitcnt lgkmcnt(3)\nv_mfma_f32_16x16x32_bf16 v[200:203], v[208:211], v[44:47], v[200:203]\nds_read_b128 v[224:227], v250 offset:23552\nv_mfma_f32_16x16x32_bf16 v[204:207], v[208:211], v[116:119], v[204:207]\ns_waitcnt vmcnt(13)\n" \
  "s_waitcnt lgkmcnt(3)\nv_mfma_f32_16x16x32_bf16 v[144:147], v[212:215], v[48:51], v[144:147]\nds_read_b128 v[208:211], v250 offset:24576\nv_mfma_f32_16x16x32_bf16 v[148:151], v[212:215], v[120:123], v[148:151]\ns_add_u32 s49, s44, 32768\nbuffer_load_dwordx4 v[140:143], v251, %[rx], s49 offen offset:3072 sc1\n" \
  "s_waitcnt lgkmcnt(3)\nv_mfma_f32_16x16x32_bf16 v[152:155], v[216:219], v[48:51], v[152:155]\nds_read_b128 v[212:215], v250 offset:25600\nv_mfma_f32_16x16x32_bf16 v[156:159], v[216:219], v[120:123], v[156:159]\ns_waitcnt lgkmcnt(3)\nv_mfma_f32_16x16x32_bf16 v[160:163], v[220:223], v[48:51], v[160:163]\n" \
  "ds_read_b128 v[216:219], v250 offset:26624\nv_mfma_f32_16x16x32_bf16 v[164:167], v[220:223], v[120:123], v[164:167]\ns_add_u32 m0, s40, 40960\ns_add_u32 s49, s41, 155648\nbuffer_load_dwordx4 v251, %[rw], s49 offen sc1 lds\ns_waitcnt lgkmcnt(3)\n" \
  "v_mfma_f32_16x16x32_bf16 v[168:171], v[224:227], v[48:51], v[168:171]\nds_read_b128 v[220:223], v250 offset:27648\nv_mfma_f32_16x16x32_bf16 v[172:175], v[224:227], v[120:123], v[172:175]\ns_waitcnt lgkmcnt(3)\nv_mfma_f32_16x16x32_bf16 v[176:179], v[208:211], v[48:51], v[176:179]\nds_read_b128 v[224:227], v250 offset:28672\n" \
  "v_mfma_f32_16x16x32_bf16 v[180:183], v[208:211], v[120:123], v[180:183]\ns_add_u32 m0, s40, 49152\ns_add_u32 s49, s41, 163840\nbuffer_load_dwordx4 v251, %[rw], s49 offen sc1 lds\ns_waitcnt lgkmcnt(3)\nv_mfma_f32_16x16x32_bf16 v[184:187], v[212:215], v[48:51], v[184:187]\n" \
  "ds_read_b128 v[208:211], v250 offset:29696\nv_mfma_f32_16x16x32_bf16 v[188:191], v[212:215], v[120:123], v[188:191]\ns_waitcnt lgkmcnt(3)\nv_mfma_f32_16x16x32_bf16 v[192:195], v[216:219], v[48:51], v[192:195]\nds_read_b128 v[212:215], v250 offset:30720\nv_mfma_f32_16x16x32_bf16 v[196:199], v[216:219], v[120:123], v[196:199]\n" \
  "s_waitcnt lgkmcnt(3)\nv_mfma_f32_16x16x32_bf16 v[200:203], v[220:223], v[48:51], v[200:203]\nds_read_b128 v[216:219], v250 offset:31744\nv_mfma_f32_16x16x32_bf16 v[204:207], v[220:223], v[120:123], v[204:207]\ns_add_u32 m0, s40, 57344\ns_add_u32 s49, s41, 172032\n" \
  "buffer_load_dwordx4 v251, %[rw], s49 offen sc1 lds\ns_waitcnt vmcnt(15)\ns_waitcnt lgkmcnt(3)\nv_mfma_f32_16x16x32_bf16 v[144:147], v[224:227], v[52:55], v[144:147]\nds_read_b128 v[220:223], v250 offset:32768\nv_mfma_f32_16x16x32_bf16 v[148:151], v[224:227], v[124:127], v[148:151]\n" \
  "s_waitcnt lgkmcnt(3)\nv_mfma_f32_16x16x32_bf16 v[152:155], v[208:211], v[52:55], v[152:155]\nds_read_b128 v[224:227], v250 offset:33792\nv_mfma_f32_16x16x32_bf16 v[156:159], v[208:211], v[124:127], v[156:159]\ns_add_u32 m0, s40, 65536\ns_add_u32 s49, s41, 180224\n" \
  "buffer_load_dwordx4 v251, %[rw], s49 offen sc1 lds\ns_waitcnt lgkmcnt(3)\nv_mfma_f32_16x16x32_bf16 v[160:163], v[212:215], v[52:55], v[160:163]\nds_read_b128 v[208:211], v250 offset:34816\nv_mfma_f32_16x16x32_bf16 v[164:167], v[212:215], v[124:127], v[164:167]\ns_waitcnt lgkmcnt(3)\n" \
  "v_mfma_f32_16x16x32_bf16 v[168:171], v[216:219], v[52:55], v[168:171]\nds_read_b128 v[212:215], v250 offset:35840\nv_mfma_f32_16x16x32_bf16 v[172:175], v[216:219], v[124:127], v[172:175]\ns_add_u32 m0, s40, 73728\ns_add_u32 s49, s41, 188416\nbuffer_load_dwordx4 v251, %[rw], s49 offen sc1 lds\n" \
  "s_waitcnt vmcnt(13)\ns_barrier\ns_waitcnt lgkmcnt(3)\nv_mfma_f32_16x16x32_bf16 v[176:179], v[220:223], v[52:55], v[176:179]\nds_read_b128 v[216:219], v248 offset:0\nv_mfma_f32_16x16x32_bf16 v[180:183], v[220:223], v[124:127], v[180:183]\n" \
  "s_waitcnt lgkmcnt(3)\nv_mfma_f32_16x16x32_bf16 v[184:187], v[224:227], v[52:55], v[184:187]\nds_read_b128 v[220:223], v248 offset:1024\nv_mfma_f32_16x16x32_bf16 v[188:191], v[224:227], v[124:127], v[188:191]\ns_add_u32 m0, s40, 81920\ns_add_u32 s49, s41, 196608\n" \
  "buffer_load_dwordx4 v251, %[rw], s49 offen sc1 lds\ns_waitcnt lgkmcnt(3)\nv_mfma_f32_16x16x32_bf16 v[192:195], v[208:211], v[52:55], v[192:195]\nds_read_b128 v[224:227], v248 offset:2048\nv_mfma_f32_16x16x32_bf16 v[196:199], v[208:211], v[124:127], v[196:199]\ns_waitcnt lgkmcnt(3)\n" \
  "v_mfma_f32_16x16x32_bf16 v[200:203], v[212:215], v[52:55], v[200:203]\nds_read_b128 v[208:211], v248 offset:3072\nv_mfma_f32_16x16x32_bf16 v[204:207], v[212:215], v[124:127], v[204:207]\ns_add_u32 m0, s40, 90112\ns_add_u32 s49, s41, 204800\nbuffer_load_dwordx4 v251, %[rw], s49 offen sc1 lds\n" \
  "s_waitcnt vmcnt(13)\ns_waitcnt lgkmcnt(3)\nv_mfma_f32_16x16x32_bf16 v[144:147], v[216:219], v[56:59], v[144:147]\nds_read_b128 v[212:215], v248 offset:4096\nv_mfma_f32_16x16x32_bf16 v[148:151], v[216:219], v[128:131], v[148:151]\ns_waitcnt lgkmcnt(3)\n" \
  "v_mfma_f32_16x16x32_bf16 v[152:155], v[220:223], v[56:59], v[152:155]\nds_read_b128 v[216:219], v248 offset:5120\nv_mfma_f32_16x16x32_bf16 v[156:159], v[220:223], v[128:131], v[156:159]\ns_add_u32 m0, s40, 98304\ns_add_u32 s49, s41, 212992\nbuffer_load_dwordx4 v251, %[rw], s49 offen sc1 lds\n" \
  "s_waitcnt lgkmcnt(3)\nv_mfma_f32_16x16x32_bf16 v[160:163], v[224:227], v[56:59], v[160:163]\nds_read_b128 v[220:223], v248 offset:6144\nv_mfma_f32_16x16x32_bf16 v[164:167], v[224:227], v[128:131], v[164:167]\ns_waitcnt lgkmcnt(3)\nv_mfma_f32_16x16x32_bf16 v[168:171], v[208:211], v[56:59], v[168:171]\n" \
  "ds_read_b128 v[224:227], v248 offset:7168\nv_mfma_f32_16x16x32_bf16 v[172:175], v[208:211], v[128:131], v[172:175]\ns_add_u32 m0, s40, 106496\ns_add_u32 s49, s41, 221184\nbuffer_load_dwordx4 v251, %[rw], s49 offen sc1 lds\ns_waitcnt lgkmcnt(3)\n" \
  "v_mfma_f32_16x16x32_bf16 v[176:179], v[212:215], v[56:59], v[176:179]\nds_read_b128 v[208:211], v248 offset:8192\nv_mfma_f32_16x16x32_bf16 v[180:183], v[212:215], v[128:131], v[180:183]\ns_waitcnt lgkmcnt(3)\nv_mfma_f32_16x16x32_bf16 v[184:187], v[216:219], v[56:59], v[184:187]\nds_read_b128 v[212:215], v248 offset:9216\n" \
  "v_mfma_f32_16x16x32_bf16 v[188:191], v[216:219], v[128:131], v[188:191]\ns_add_u32 m0, s40, 114688\ns_add_u32 s49, s41, 229376\nbuffer_load_dwordx4 v251, %[rw], s49 offen sc1 lds\ns_waitcnt lgkmcnt(3)\nv_mfma_f32_16x16x32_bf16 v[192:195], v[220:223], v[56:59], v[192:195]\n" \
  "ds_read_b128 v[216:219], v248 offset:10240\nv_mfma_f32_16x16x32_bf16 v[196:199], v[220:223], v[128:131], v[196:199]\ns_waitcnt lgkmcnt(3)\nv_mfma_f32_16x16x32_bf16 v[200:203], v[224:227], v[56:59], v[200:203]\nds_read_b128 v[220:223], v248 offset:11264\nv_mfma_f32_16x16x32_bf16 v[204:207], v[224:227], v[128:131], v[204:207]\n" \
  "s_add_u32 m0, s40, 122880\ns_add_u32 s49, s41, 237568\nbuffer_load_dwordx4 v251, %[rw], s49 offen sc1 lds\ns_waitcnt vmcnt(15)\ns_waitcnt lgkmcnt(3)\nv_mfma_f32_16x16x32_bf16 v[144:147], v[208:211], v[60:63], v[144:147]\n" \
  "ds_read_b128 v[224:227], v248 offset:12288\nv_mfma_f32_16x16x32_bf16 v[148:151], v[208:211], v[132:135], v[148:151]\ns_waitcnt lgkmcnt(3)\nv_mfma_f32_16x16x32_bf16 v[152:155], v[212:215], v[60:63], v[152:155]\nds_read_b128 v[208:211], v248 offset:13312\nv_mfma_f32_16x16x32_bf16 v[156:159], v[212:215], v[132:135], v[156:159]\n" \
  "s_add_u32 m0, s40, 131072\ns_add_u32 s49, s41, 245760\nbuffer_load_dwordx4 v251, %[rw], s49 offen sc1 lds\ns_waitcnt vmcnt(14)\ns_waitcnt lgkmcnt(3)\nv_mfma_f32_16x16x32_bf16 v[144:147], v[216:219], v[64:67], v[144:147]\n" \
  "ds_read_b128 v[212:215], v248 offset:14336\nv_mfma_f32_16x16x32_bf16 v[148:151], v[216:219], v[136:139], v[148:151]\ns_waitcnt lgkmcnt(3)\nv_mfma_f32_16x16x32_bf16 v[152:155], v[220:223], v[64:67], v[152:155]\nds_read_b128 v[216:219], v248 offset:15360\nv_mfma_f32_16x16x32_bf16 v[156:159], v[220:223], v[136:139], v[156:159]\n" \
  "s_waitcnt vmcnt(12)\ns_waitcnt lgkmcnt(3)\nv_mfma_f32_16x16x32_bf16 v[144:147], v[224:227], v[68:71], v[144:147]\nds_read_b128 v[220:223], v248 offset:16384\nv_mfma_f32_16x16x32_bf16 v[148:151], v[224:227], v[140:143], v[148:151]\ns_waitcnt lgkmcnt(3)\n" \
  "v_mfma_f32_16x16x32_bf16 v[152:155], v[208:211], v[68:71], v[152:155]\nds_read_b128 v[224:227], v248 offset:17408\nv_mfma_f32_16x16x32_bf16 v[156:159], v[208:211], v[140:143], v[156:159]\ns_waitcnt lgkmcnt(3)\nv_mfma_f32_16x16x32_bf16 v[160:163], v[212:215], v[60:63], v[160:163]\nds_read_b128 v[208:211], v248 offset:18432\n" \
  "v_mfma_f32_16x16x32_bf16 v[164:167], v[212:215], v[132:135], v[164:167]\ns_waitcnt lgkmcnt(3)\nv_mfma_f32_16x16x32_bf16 v[168:171], v[216:219], v[60:63], v[168:171]\nds_read_b128 v[212:215], v248 offset:19456\nv_mfma_f32_16x16x32_bf16 v[172:175], v[216:219], v[132:135], v[172:175]\ns_waitcnt lgkmcnt(3)\n" \
  "v_mfma_f32_16x16x32_bf16 v[160:163], v[220:223], v[64:67], v[160:163]\nds_read_b128 v[216:219], v248 offset:20480\nv_mfma_f32_16x16x32_bf16 v[164:167], v[220:223], v[136:139], v[164:167]\ns_waitcnt lgkmcnt(3)\nv_mfma_f32_16x16x32_bf16 v[168:171], v[224:227], v[64:67], v[168:171]\nds_read_b128 v[220:223], v248 offset:21504\n" \
  "v_max_f32 v144, 0, v144\nv_mfma_f32_16x16x32_bf16 v[172:175], v[224:227], v[136:139], v[172:175]\nv_max_f32 v145, 0, v145\ns_waitcnt lgkmcnt(3)\nv_mfma_f32_16x16x32_bf16 v[160:163], v[208:211], v[68:71], v[160:163]\nds_read_b128 v[224:227], v248 offset:22528\n" \
  "v_max_f32 v146, 0, v146\nv_mfma_f32_16x16x32_bf16 v[164:167], v[208:211], v[140:143], v[164:167]\nv_max_f32 v147, 0, v147\ns_waitcnt lgkmcnt(3)\nv_mfma_f32_16x16x32_bf16 v[168:171], v[212:215], v[68:71], v[168:171]\nds_read_b128 v[208:211], v248 offset:23552\n" \
  "v_mul_f32 v144, v144, v152\nv_mfma_f32_16x16x32_bf16 v[172:175], v[212:215], v[140:143], v[172:175]\nv_mul_f32 v145, v145, v153\ns_waitcnt lgkmcnt(3)\nv_mfma_f32_16x16x32_bf16 v[176:179], v[216:219], v[60:63], v[176:179]\nds_read_b128 v[212:215], v248 offset:24576\n" \
  "v_mul_f32 v146, v146, v154\nv_mfma_f32_16x16x32_bf16 v[180:183], v[216:219], v[132:135], v[180:183]\nv_mul_f32 v147, v147, v155\ns_waitcnt lgkmcnt(3)\nv_mfma_f32_16x16x32_bf16 v[184:187], v[220:223], v[60:63], v[184:187]\nds_read_b128 v[216:219], v248 offset:25600\n" \
  "v_max_f32 v148, 0, v148\nv_mfma_f32_16x16x32_bf16 v[188:191], v[220:223], v[132:135], v[188:191]\nv_max_f32 v149, 0, v149\ns_waitcnt lgkmcnt(3)\nv_mfma_f32_16x16x32_bf16 v[176:179], v[224:227], v[64:67], v[176:179]\nds_read_b128 v[220:223], v248 offset:26624\n" \
  "v_max_f32 v150, 0, v150\nv_mfma_f32_16x16x32_bf16 v[180:183], v[224:227], v[136:139], v[180:183]\nv_max_f32 v151, 0, v151\ns_waitcnt lgkmcnt(3)\nv_mfma_f32_16x16x32_bf16 v[184:187], v[208:211], v[64:67], v[184:187]\nds_read_b128 v[224:227], v248 offset:27648\n" \
  "v_mul_f32 v148, v148, v156\nv_mfma_f32_16x16x32_bf16 v[188:191], v[208:211], v[136:139], v[188:191]\nv_mul_f32 v149, v149, v157\ns_waitcnt lgkmcnt(3)\nv_mfma_f32_16x16x32_bf16 v[176:179], v[212:215], v[68:71], v[176:179]\nds_read_b128 v[208:211], v248 offset:28672\n" \
  "v_mul_f32 v150, v150, v158\nv_mfma_f32_16x16x32_bf16 v[180:183], v[212:215], v[140:143], v[180:183]\nv_mul_f32 v151, v151, v159\ns_waitcnt lgkmcnt(3)\nv_mfma_f32_16x16x32_bf16 v[184:187], v[216:219], v[68:71], v[184:187]\nds_read_b128 v[212:215], v248 offset:29696\n" \
  "s_nop 1\nv_mfma_f32_16x16x32_bf16 v[188:191], v[216:219], v[140:143], v[188:191]\nv_permlane32_swap_b32 v144, v148\ns_waitcnt lgkmcnt(3)\nv_mfma_f32_16x16x32_bf16 v[192:195], v[220:223], v[60:63], v[192:195]\nds_read_b128 v[216:219], v248 offset:30720\n" \
  "v_permlane32_swap_b32 v145, v149\nv_mfma_f32_16x16x32_bf16 v[196:199], v[220:223], v[132:135], v[196:199]\nv_permlane32_swap_b32 v146, v150\ns_waitcnt lgkmcnt(3)\nv_mfma_f32_16x16x32_bf16 v[200:203], v[224:227], v[60:63], v[200:203]\nds_read_b128 v[220:223], v248 offset:31744\n" \
  "v_permlane32_swap_b32 v147, v151\nv_mfma_f32_16x16x32_bf16 v[204:207], v[224:227], v[132:135], v[204:207]\nv_cvt_pk_bf16_f32 v240, v144, v148\ns_waitcnt vmcnt(8)\ns_barrier\ns_waitcnt lgkmcnt(3)\n" \
  "v_mfma_f32_16x16x32_bf16 v[192:195], v[208:211], v[64:67], v[192:195]\nds_read_b128 v[224:227], v248 offset:32768\nv_cvt_pk_bf16_f32 v241, v145, v149\nv_mfma_f32_16x16x32_bf16 v[196:199], v[208:211], v[136:139], v[196:199]\ns_add_u32 m0, s40, 139264\ns_add_u32 s49, s41, 253952\n" \
  "buffer_load_dwordx4 v251, %[rw], s49 offen sc1 lds\nv_cvt_pk_bf16_f32 v242, v146, v150\ns_waitcnt lgkmcnt(3)\nv_mfma_f32_16x16x32_bf16 v[200:203], v[212:215], v[64:67], v[200:203]\nds_read_b128 v[208:211], v248 offset:33792\nv_cvt_pk_bf16_f32 v243, v147, v151\n" \
  "v_mfma_f32_16x16x32_bf16 v[204:207], v[212:215], v[136:139], v[204:207]\nbuffer_store_dwordx4 v[240:243], v252, %[rh], s46 offen sc1\ns_waitcnt lgkmcnt(3)\nv_mfma_f32_16x16x32_bf16 v[192:195], v[216:219], v[68:71], v[192:195]\nds_read_b128 v[212:215], v248 offset:34816\nv_max_f32 v160, 0, v160\n" \
  "v_mfma_f32_16x16x32_bf16 v[196:199], v[216:219], v[140:143], v[196:199]\ns_add_u32 m0, s40, 0\ns_add_u32 s49, s41, 262144\nbuffer_load_dwordx4 v251, %[rw], s49 offen sc1 lds\nv_max_f32 v161, 0, v161\ns_waitcnt lgkmcnt(3)\n" \
  "v_mfma_f32_16x16x32_bf16 v[200:203], v[220:223], v[68:71], v[200:203]\nds_read_b128 v[216:219], v248 offset:35840\nv_max_f32 v162, 0, v162\nv_mfma_f32_16x16x32_bf16 v[204:207], v[220:223], v[140:143], v[204:207]\nv_max_f32 v163, 0, v163\ns_waitcnt lgkmcnt(3)\n" \
  "v_mfma_f32_16x16x32_bf16 v[144:147], v[224:227], v[0:3], 0\nds_read_b128 v[220:223], v248 offset:36864\nv_mul_f32 v232, v160, v168\nv_mfma_f32_16x16x32_bf16 v[148:151], v[224:227], v[72:75], 0\ns_add_u32 m0, s40, 8192\ns_add_u32 s49, s41, 270336\n" \
  "buffer_load_dwordx4 v251, %[rw], s49 offen sc1 lds\nv_mul_f32 v233, v161, v169\ns_waitcnt lgkmcnt(3)\nv_mfma_f32_16x16x32_bf16 v[152:155], v[208:211], v[0:3], 0\nds_read_b128 v[224:227], v248 offset:37888\nv_mul_f32 v234, v162, v170\n" \
  "v_mfma_f32_16x16x32_bf16 v[156:159], v[208:211], v[72:75], 0\nv_mul_f32 v235, v163, v171\ns_waitcnt lgkmcnt(3)\nv_mfma_f32_16x16x32_bf16 v[144:147], v[212:215], v[4:7], v[144:147]\nds_read_b128 v[208:211], v248 offset:38912\nv_max_f32 v164, 0, v164\n" \
  "v_mfma_f32_16x16x32_bf16 v[148:151], v[212:215], v[76:79], v[148:151]\ns_add_u32 m0, s40, 16384\ns_add_u32 s49, s41, 278528\nbuffer_load_dwordx4 v251, %[rw], s49 offen sc1 lds\nv_max_f32 v165, 0, v165\ns_waitcnt lgkmcnt(3)\n" \
  "v_mfma_f32_16x16x32_bf16 v[152:155], v[216:219], v[4:7], v[152:155]\nds_read_b128 v[212:215], v248 offset:39936\nv_max_f32 v166, 0, v166\nv_mfma_f32_16x16x32_bf16 v[156:159], v[216:219], v[76:79], v[156:159]\nv_max_f32 v167, 0, v167\ns_waitcnt lgkmcnt(3)\n" \
  "v_mfma_f32_16x16x32_bf16 v[144:147], v[220:223], v[8:11], v[144:147]\nds_read_b128 v[216:219], v248 offset:40960\nv_mul_f32 v236, v164, v172\nv_mfma_f32_16x16x32_bf16 v[148:151], v[220:223], v[80:83], v[148:151]\nv_mul_f32 v237, v165, v173\ns_waitcnt lgkmcnt(3)\n" \
  "v_mfma_f32_16x16x32_bf16 v[152:155], v[224:227], v[8:11], v[152:155]\nds_read_b128 v[220:223], v248 offset:41984\nv_mul_f32 v238, v166, v174\nv_mfma_f32_16x16x32_bf16 v[156:159], v[224:227], v[80:83], v[156:159]\nv_mul_f32 v239, v167, v175\ns_waitcnt lgkmcnt(3)\n" \
  "v_mfma_f32_16x16x32_bf16 v[144:147], v[208:211], v[12:15], v[144:147]\nds_read_b128 v[224:227], v248 offset:43008\nv_max_f32 v176, 0, v176\nv_mfma_f32_16x16x32_bf16 v[148:151], v[208:211], v[84:87], v[148:151]\nv_max_f32 v177, 0, v177\ns_waitcnt lgkmcnt(3)\n" \
  "v_mfma_f32_16x16x32_bf16 v[152:155], v[212:215], v[12:15], v[152:155]\nds_read_b128 v[208:211], v248 offset:44032\nv_max_f32 v178, 0, v178\nv_mfma_f32_16x16x32_bf16 v[156:159], v[212:215], v[84:87], v[156:159]\nv_max_f32 v179, 0, v179\ns_waitcnt lgkmcnt(3)\n" \
  "v_mfma_f32_16x16x32_bf16 v[144:147], v[216:219], v[16:19], v[144:147]\nds_read_b128 v[212:215], v248 offset:45056\nv_mul_f32 v176, v176, v184\nv_mfma_f32_16x16x32_bf16 v[148:151], v[216:219], v[88:91], v[148:151]\nv_mul_f32 v177, v177, v185\ns_waitcnt lgkmcnt(3)\n" \
  "v_mfma_f32_16x16x32_bf16 v[152:155], v[220:223], v[16:19], v[152:155]\nds_read_b128 v[216:219], v248 offset:46080\nv_mul_f32 v178, v178, v186\nv_mfma_f32_16x16x32_bf16 v[156:159], v[220:223], v[88:91], v[156:159]\nv_mul_f32 v179, v179, v187\ns_waitcnt lgkmcnt(3)\n" \
  "v_mfma_f32_16x16x32_bf16 v[144:147], v[224:227], v[20:23], v[144:147]\nds_read_b128 v[220:223], v248 offset:47104\nv_cvt_pk_bf16_f32 v240, v232, v176\nv_mfma_f32_16x16x32_bf16 v[148:151], v[224:227], v[92:95], v[148:151]\nv_cvt_pk_bf16_f32 v241, v233, v177\ns_waitcnt lgkmcnt(3)\n" \
  "v_mfma_f32_16x16x32_bf16 v[152:155], v[208:211], v[20:23], v[152:155]\nds_read_b128 v[224:227], v248 offset:48128\nv_cvt_pk_bf16_f32 v242, v234, v178\nv_mfma_f32_16x16x32_bf16 v[156:159], v[208:211], v[92:95], v[156:159]\nv_cvt_pk_bf16_f32 v243, v235, v179\ns_waitcnt lgkmcnt(3)\n" \
  "v_mfma_f32_16x16x32_bf16 v[144:147], v[212:215], v[24:27], v[144:147]\nds_read_b128 v[208:211], v248 offset:49152\ns_add_u32 s49, s47, 0\nbuffer_store_dwordx4 v[240:243], v251, %[rh], s49 offen sc1\nv_mfma_f32_16x16x32_bf16 v[148:151], v[212:215], v[96:99], v[148:151]\nv_max_f32 v180, 0, v180\n" \
  "s_waitcnt lgkmcnt(3)\nv_mfma_f32_16x16x32_bf16 v[152:155], v[216:219], v[24:27], v[152:155]\nds_read_b128 v[212:215], v248 offset:50176\nv_max_f32 v181, 0, v181\nv_mfma_f32_16x16x32_bf16 v[156:159], v[216:219], v[96:99], v[156:159]\nv_max_f32 v182, 0, v182\n" \
  "s_waitcnt lgkmcnt(3)\nv_mfma_f32_16x16x32_bf16 v[144:147], v[220:223], v[28:31], v[144:147]\nds_read_b128 v[216:219], v248 offset:51200\nv_max_f32 v183, 0, v183\nv_mfma_f32_16x16x32_bf16 v[148:151], v[220:223], v[100:103], v[148:151]\nv_mul_f32 v180, v180, v188\n" \
  "s_waitcnt lgkmcnt(3)\nv_mfma_f32_16x16x32_bf16 v[152:155], v[224:227], v[28:31], v[152:155]\nds_read_b128 v[220:223], v248 offset:52224\nv_mul_f32 v181, v181, v189\nv_mfma_f32_16x16x32_bf16 v[156:159], v[224:227], v[100:103], v[156:159]\nv_mul_f32 v182, v182, v190\n" \
  "s_waitcnt lgkmcnt(3)\nv_mfma_f32_16x16x32_bf16 v[144:147], v[208:211], v[32:35], v[144:147]\nds_read_b128 v[224:227], v248 offset:53248\nv_mul_f32 v183, v183, v191\nv_mfma_f32_16x16x32_bf16 v[148:151], v[208:211], v[104:107], v[148:151]\nv_cvt_pk_bf16_f32 v244, v236, v180\n" \
  "s_waitcnt lgkmcnt(3)\nv_mfma_f32_16x16x32_bf16 v[152:155], v[212:215], v[32:35], v[152:155]\nds_read_b128 v[208:211], v248 offset:54272\nv_cvt_pk_bf16_f32 v245, v237, v181\nv_mfma_f32_16x16x32_bf16 v[156:159], v[212:215], v[104:107], v[156:159]\nv_cvt_pk_bf16_f32 v246, v238, v182\n" \
  "s_waitcnt lgkmcnt(3)\nv_mfma_f32_16x16x32_bf16 v[144:147], v[216:219], v[36:39], v[144:147]\nds_read_b128 v[212:215], v249 offset:0\nv_cvt_pk_bf16_f32 v247, v239, v183\nv_mfma_f32_16x16x32_bf16 v[148:151], v[216:219], v[108:111], v[148:151]\ns_add_u32 s49, s47, 147456\n" \
  "buffer_store_dwordx4 v[244:247], v251, %[rh], s49 offen sc1\ns_waitcnt lgkmcnt(3)\nv_mfma_f32_16x16x32_bf16 v[152:155], v[220:223], v[36:39], v[152:155]\nds_read_b128 v[216:219], v249 offset:1024\nv_max_f32 v192, 0, v192\nv_mfma_f32_16x16x32_bf16 v[156:159], v[220:223], v[108:111], v[156:159]\n" \
  "v_max_f32 v193, 0, v193\ns_waitcnt lgkmcnt(3)\nv_mfma_f32_16x16x32_bf16 v[144:147], v[224:227], v[40:43], v[144:147]\nds_read_b128 v[220:223], v249 offset:2048\nv_max_f32 v194, 0, v194\nv_mfma_f32_16x16x32_bf16 v[148:151], v[224:227], v[112:115], v[148:151]\n" \
  "v_max_f32 v195, 0, v195\ns_waitcnt lgkmcnt(3)\nv_mfma_f32_16x16x32_bf16 v[152:155], v[208:211], v[40:43], v[152:155]\nds_read_b128 v[224:227], v249 offset:3072\nv_mul_f32 v232, v192, v200\nv_mfma_f32_16x16x32_bf16 v[156:159], v[208:211], v[112:115], v[156:159]\n" \
  "v_mul_f32 v233, v193, v201\ns_waitcnt lgkmcnt(3)\nv_mfma_f32_16x16x32_bf16 v[144:147], v[212:215], v[44:47], v[144:147]\nds_read_b128 v[208:211], v249 offset:4096\nv_mul_f32 v234, v194, v202\nv_mfma_f32_16x16x32_bf16 v[148:151], v[212:215], v[116:119], v[148:151]\n" \
  "v_mul_f32 v235, v195, v203\ns_waitcnt lgkmcnt(3)\nv_mfma_f32_16x16x32_bf16 v[152:155], v[216:219], v[44:47], v[152:155]\nds_read_b128 v[212:215], v249 offset:5120\nv_max_f32 v196, 0, v196\nv_mfma_f32_16x16x32_bf16 v[156:159], v[216:219], v[116:119], v[156:159]\n" \
  "v_max_f32 v197, 0, v197\ns_waitcnt lgkmcnt(3)\nv_mfma_f32_16x16x32_bf16 v[144:147], v[220:223], v[48:51], v[144:147]\nds_read_b128 v[216:219], v249 offset:6144\nv_max_f32 v198, 0, v198\nv_mfma_f32_16x16x32_bf16 v[148:151], v[220:223], v[120:123], v[148:151]\n" \
  "v_max_f32 v199, 0, v199\ns_waitcnt lgkmcnt(3)\nv_mfma_f32_16x16x32_bf16 v[152:155], v[224:227], v[48:51], v[152:155]\nds_read_b128 v[220:223], v249 offset:7168\nv_mul_f32 v236, v196, v204\nv_mfma_f32_16x16x32_bf16 v[156:159], v[224:227], v[120:123], v[156:159]\n" \
  "v_mul_f32 v237, v197, v205\ns_waitcnt lgkmcnt(3)\nv_mfma_f32_16x16x32_bf16 v[144:147], v[208:211], v[52:55], v[144:147]\nds_read_b128 v[224:227], v249 offset:8192\nv_mul_f32 v238, v198, v206\nv_mfma_f32_16x16x32_bf16 v[148:151], v[208:211], v[124:127], v[148:151]\n" \
  "v_mul_f32 v239, v199, v207\ns_waitcnt lgkmcnt(3)\nv_mfma_f32_16x16x32_bf16 v[152:155], v[212:215], v[52:55], v[152:155]\nds_read_b128 v[208:211], v249 offset:9216\nv_mfma_f32_16x16x32_bf16 v[156:159], v[212:215], v[124:127], v[156:159]\ns_waitcnt lgkmcnt(3)\n" \
  "v_mfma_f32_16x16x32_bf16 v[144:147], v[216:219], v[56:59], v[144:147]\nds_read_b128 v[212:215], v249 offset:10240\nv_mfma_f32_16x16x32_bf16 v[148:151], v[216:219], v[128:131], v[148:151]\ns_waitcnt lgkmcnt(3)\nv_mfma_f32_16x16x32_bf16 v[152:155], v[220:223], v[56:59], v[152:155]\nds_read_b128 v[216:219], v249 offset:11264\n" \
  "v_mfma_f32_16x16x32_bf16 v[156:159], v[220:223], v[128:131], v[156:159]\ns_waitcnt lgkmcnt(3)\nv_mfma_f32_16x16x32_bf16 v[144:147], v[224:227], v[60:63], v[144:147]\nds_read_b128 v[220:223], v249 offset:12288\nv_mfma_f32_16x16x32_bf16 v[148:151], v[224:227], v[132:135], v[148:151]\ns_waitcnt lgkmcnt(3)\n" \
  "v_mfma_f32_16x16x32_bf16 v[152:155], v[208:211], v[60:63], v[152:155]\nds_read_b128 v[224:227], v249 offset:13312\nv_mfma_f32_16x16x32_bf16 v[156:159], v[208:211], v[132:135], v[156:159]\ns_waitcnt vmcnt(10)\ns_barrier\ns_waitcnt lgkmcnt(3)\n" \
  "v_mfma_f32_16x16x32_bf16 v[144:147], v[212:215], v[64:67], v[144:147]\nds_read_b128 v[208:211], v249 offset:14336\nv_mfma_f32_16x16x32_bf16 v[148:151], v[212:215], v[136:139], v[148:151]\ns_add_u32 m0, s40, 24576\ns_add_u32 s49, s41, 286720\nbuffer_load_dwordx4 v251, %[rw], s49 offen sc1 lds\n" \
  "s_waitcnt lgkmcnt(3)\nv_mfma_f32_16x16x32_bf16 v[152:155], v[216:219], v[64:67], v[152:155]\nds_read_b128 v[212:215], v249 offset:15360\nv_mfma_f32_16x16x32_bf16 v[156:159], v[216:219], v[136:139], v[156:159]\ns_waitcnt lgkmcnt(3)\nv_mfma_f32_16x16x32_bf16 v[144:147], v[220:223], v[68:71], v[144:147]\n" \
  "ds_read_b128 v[216:219], v249 offset:16384\nv_mfma_f32_16x16x32_bf16 v[148:151], v[220:223], v[140:143], v[148:151]\ns_add_u32 m0, s40, 32768\ns_add_u32 s49, s41, 294912\nbuffer_load_dwordx4 v251, %[rw], s49 offen sc1 lds\ns_waitcnt lgkmcnt(3)\n" \
  "v_mfma_f32_16x16x32_bf16 v[152:155], v[224:227], v[68:71], v[152:155]\nds_read_b128 v[220:223], v249 offset:17408\nv_mfma_f32_16x16x32_bf16 v[156:159], v[224:227], v[140:143], v[156:159]\ns_waitcnt lgkmcnt(3)\nv_mfma_f32_16x16x32_bf16 v[160:163], v[208:211], v[0:3], 0\nds_read_b128 v[224:227], v249 offset:18432\n" \
  "v_mfma_f32_16x16x32_bf16 v[164:167], v[208:211], v[72:75], 0\ns_add_u32 m0, s40, 40960\ns_add_u32 s49, s41, 303104\nbuffer_load_dwordx4 v251, %[rw], s49 offen sc1 lds\ns_waitcnt lgkmcnt(3)\nv_mfma_f32_16x16x32_bf16 v[168:171], v[212:215], v[0:3], 0\n" \
  "ds_read_b128 v[208:211], v249 offset:19456\nv_mfma_f32_16x16x32_bf16 v[172:175], v[212:215], v[72:75], 0\ns_waitcnt lgkmcnt(3)\nv_mfma_f32_16x16x32_bf16 v[160:163], v[216:219], v[4:7], v[160:163]\nds_read_b128 v[212:215], v249 offset:20480\nv_mfma_f32_16x16x32_bf16 v[164:167], v[216:219], v[76:79], v[164:167]\n" \
  "s_add_u32 m0, s40, 49152\ns_add_u32 s49, s41, 311296\nbuffer_load_dwordx4 v251, %[rw], s49 offen sc1 lds\ns_waitcnt lgkmcnt(3)\nv_mfma_f32_16x16x32_bf16 v[168:171], v[220:223], v[4:7], v[168:171]\nds_read_b128 v[216:219], v249 offset:21504\n" \
  "v_max_f32 v144, 0, v144\nv_mfma_f32_16x16x32_bf16 v[172:175], v[220:223], v[76:79], v[172:175]\nv_max_f32 v145, 0, v145\ns_waitcnt lgkmcnt(3)\nv_mfma_f32_16x16x32_bf16 v[160:163], v[224:227], v[8:11], v[160:163]\nds_read_b128 v[220:223], v249 offset:22528\n" \
  "v_max_f32 v146, 0, v146\nv_mfma_f32_16x16x32_bf16 v[164:167], v[224:227], v[80:83], v[164:167]\ns_add_u32 m0, s40, 57344\ns_add_u32 s49, s41, 319488\nbuffer_load_dwordx4 v251, %[rw], s49 offen sc1 lds\nv_max_f32 v147, 0, v147\n" \
  "s_waitcnt lgkmcnt(3)\nv_mfma_f32_16x16x32_bf16 v[168:171], v[208:211], v[8:11], v[168:171]\nds_read_b128 v[224:227], v249 offset:23552\nv_mul_f32 v144, v144, v152\nv_mfma_f32_16x16x32_bf16 v[172:175], v[208:211], v[80:83], v[172:175]\nv_mul_f32 v145, v145, v153\n" \
  "s_waitcnt lgkmcnt(3)\nv_mfma_f32_16x16x32_bf16 v[160:163], v[212:215], v[12:15], v[160:163]\nds_read_b128 v[208:211], v249 offset:24576\nv_mul_f32 v146, v146, v154\nv_mfma_f32_16x16x32_bf16 v[164:167], v[212:215], v[84:87], v[164:167]\nv_mul_f32 v147, v147, v155\n" \
  "s_waitcnt lgkmcnt(3)\nv_mfma_f32_16x16x32_bf16 v[168:171], v[216:219], v[12:15], v[168:171]\nds_read_b128 v[212:215], v249 offset:25600\nv_cvt_pk_bf16_f32 v240, v232, v144\nv_mfma_f32_16x16x32_bf16 v[172:175], v[216:219], v[84:87], v[172:175]\nv_cvt_pk_bf16_f32 v241, v233, v145\n" \
  "s_waitcnt lgkmcnt(3)\nv_mfma_f32_16x16x32_bf16 v[160:163], v[220:223], v[16:19], v[160:163]\nds_read_b128 v[216:219], v249 offset:26624\nv_cvt_pk_bf16_f32 v242, v234, v146\nv_mfma_f32_16x16x32_bf16 v[164:167], v[220:223], v[88:91], v[164:167]\nv_cvt_pk_bf16_f32 v243, v235, v147\n" \
  "s_waitcnt lgkmcnt(3)\nv_mfma_f32_16x16x32_bf16 v[168:171], v[224:227], v[16:19], v[168:171]\nds_read_b128 v[220:223], v249 offset:27648\ns_add_u32 s49, s47, 12288\nbuffer_store_dwordx4 v[240:243], v251, %[rh], s49 offen sc1\nv_mfma_f32_16x16x32_bf16 v[172:175], v[224:227], v[88:91], v[172:175]\n" \
  "v_max_f32 v148, 0, v148\ns_waitcnt lgkmcnt(3)\nv_mfma_f32_16x16x32_bf16 v[160:163], v[208:211], v[20:23], v[160:163]\nds_read_b128 v[224:227], v249 offset:28672\nv_max_f32 v149, 0, v149\nv_mfma_f32_16x16x32_bf16 v[164:167], v[208:211], v[92:95], v[164:167]\n" \
  "v_max_f32 v150, 0, v150\ns_waitcnt lgkmcnt(3)\nv_mfma_f32_16x16x32_bf16 v[168:171], v[212:215], v[20:23], v[168:171]\nds_read_b128 v[208:211], v249 offset:29696\nv_max_f32 v151, 0, v151\nv_mfma_f32_16x16x32_bf16 v[172:175], v[212:215], v[92:95], v[172:175]\n" \
  "v_mul_f32 v148, v148, v156\ns_waitcnt lgkmcnt(3)\nv_mfma_f32_16x16x32_bf16 v[160:163], v[216:219], v[24:27], v[160:163]\nds_read_b128 v[212:215], v249 offset:30720\nv_mul_f32 v149, v149, v157\nv_mfma_f32_16x16x32_bf16 v[164:167], v[216:219], v[96:99], v[164:167]\n" \
  "v_mul_f32 v150, v150, v158\ns_waitcnt lgkmcnt(3)\nv_mfma_f32_16x16x32_bf16 v[168:171], v[220:223], v[24:27], v[168:171]\nds_read_b128 v[216:219], v249 offset:31744\nv_mul_f32 v151, v151, v159\nv_mfma_f32_16x16x32_bf16 v[172:175], v[220:223], v[96:99], v[172:175]\n" \
  "v_cvt_pk_bf16_f32 v244, v236, v148\ns_waitcnt lgkmcnt(3)\nv_mfma_f32_16x16x32_bf16 v[160:163], v[224:227], v[28:31], v[160:163]\nds_read_b128 v[220:223], v249 offset:32768\nv_cvt_pk_bf16_f32 v245, v237, v149\nv_mfma_f32_16x16x32_bf16 v[164:167], v[224:227], v[100:103], v[164:167]\n" \
  "v_cvt_pk_bf16_f32 v246, v238, v150\ns_waitcnt lgkmcnt(3)\nv_mfma_f32_16x16x32_bf16 v[168:171], v[208:211], v[28:31], v[168:171]\nds_read_b128 v[224:227], v249 offset:33792\nv_cvt_pk_bf16_f32 v247, v239, v151\nv_mfma_f32_16x16x32_bf16 v[172:175], v[208:211], v[100:103], v[172:175]\n" \
  "s_add_u32 s49, s47, 159744\nbuffer_store_dwordx4 v[244:247], v251, %[rh], s49 offen sc1\ns_waitcnt lgkmcnt(3)\nv_mfma_f32_16x16x32_bf16 v[160:163], v[212:215], v[32:35], v[160:163]\nds_read_b128 v[208:211], v249 offset:34816\nv_mfma_f32_16x16x32_bf16 v[164:167], v[212:215], v[104:107], v[164:167]\n" \
  "s_waitcnt lgkmcnt(3)\nv_mfma_f32_16x16x32_bf16 v[168:171], v[216:219], v[32:35], v[168:171]\nds_read_b128 v[212:215], v249 offset:35840\nv_mfma_f32_16x16x32_bf16 v[172:175], v[216:219], v[104:107], v[172:175]\ns_waitcnt lgkmcnt(3)\nv_mfma_f32_16x16x32_bf16 v[160:163], v[220:223], v[36:39], v[160:163]\n" \
  "ds_read_b128 v[216:219], v249 offset:36864\nv_mfma_f32_16x16x32_bf16 v[164:167], v[220:223], v[108:111], v[164:167]\ns_waitcnt lgkmcnt(3)\nv_mfma_f32_16x16x32_bf16 v[168:171], v[224:227], v[36:39], v[168:171]\nds_read_b128 v[220:223], v249 offset:37888\nv_mfma_f32_16x16x32_bf16 v[172:175], v[224:227], v[108:111], v[172:175]\n" \
  "s_waitcnt vmcnt(7)\ns_mov_b64 exec, 1\nglobal_atomic_add v230, v229, %[cout] offset:0\ns_mov_b64 exec, -1\ns_waitcnt lgkmcnt(3)\nv_mfma_f32_16x16x32_bf16 v[160:163], v[208:211], v[40:43], v[160:163]\n" \
  "ds_read_b128 v[224:227], v249 offset:38912\nv_mfma_f32_16x16x32_bf16 v[164:167], v[208:211], v[112:115], v[164:167]\ns_waitcnt lgkmcnt(3)\nv_mfma_f32_16x16x32_bf16 v[168:171], v[212:215], v[40:43], v[168:171]\nds_read_b128 v[208:211], v249 offset:39936\nv_mfma_f32_16x16x32_bf16 v[172:175], v[212:215], v[112:115], v[172:175]\n" \
  "s_waitcnt lgkmcnt(3)\nv_mfma_f32_16x16x32_bf16 v[160:163], v[216:219], v[44:47], v[160:163]\nds_read_b128 v[212:215], v249 offset:40960\nv_mfma_f32_16x16x32_bf16 v[164:167], v[216:219], v[116:119], v[164:167]\ns_waitcnt lgkmcnt(3)\nv_mfma_f32_16x16x32_bf16 v[168:171], v[220:223], v[44:47], v[168:171]\n" \
  "ds_read_b128 v[216:219], v249 offset:41984\nv_mfma_f32_16x16x32_bf16 v[172:175], v[220:223], v[116:119], v[172:175]\ns_waitcnt lgkmcnt(3)\nv_mfma_f32_16x16x32_bf16 v[160:163], v[224:227], v[48:51], v[160:163]\nds_read_b128 v[220:223], v249 offset:43008\nv_mfma_f32_16x16x32_bf16 v[164:167], v[224:227], v[120:123], v[164:167]\n" \
  "s_waitcnt lgkmcnt(3)\nv_mfma_f32_16x16x32_bf16 v[168:171], v[208:211], v[48:51], v[168:171]\nds_read_b128 v[224:227], v249 offset:44032\nv_mfma_f32_16x16x32_bf16 v[172:175], v[208:211], v[120:123], v[172:175]\ns_waitcnt lgkmcnt(3)\nv_mfma_f32_16x16x32_bf16 v[160:163], v[212:215], v[52:55], v[160:163]\n" \
  "ds_read_b128 v[208:211], v249 offset:45056\nv_mfma_f32_16x16x32_bf16 v[164:167], v[212:215], v[124:127], v[164:167]\ns_waitcnt lgkmcnt(3)\nv_mfma_f32_16x16x32_bf16 v[168:171], v[216:219], v[52:55], v[168:171]\nds_read_b128 v[212:215], v249 offset:46080\nv_mfma_f32_16x16x32_bf16 v[172:175], v[216:219], v[124:127], v[172:175]\n" \
  "s_waitcnt lgkmcnt(3)\nv_mfma_f32_16x16x32_bf16 v[160:163], v[220:223], v[56:59], v[160:163]\nds_read_b128 v[216:219], v249 offset:47104\nv_mfma_f32_16x16x32_bf16 v[164:167], v[220:223], v[128:131], v[164:167]\ns_waitcnt lgkmcnt(3)\nv_mfma_f32_16x16x32_bf16 v[168:171], v[224:227], v[56:59], v[168:171]\n" \
  "ds_read_b128 v[220:223], v249 offset:48128\nv_mfma_f32_16x16x32_bf16 v[172:175], v[224:227], v[128:131], v[172:175]\ns_waitcnt lgkmcnt(3)\nv_mfma_f32_16x16x32_bf16 v[160:163], v[208:211], v[60:63], v[160:163]\nds_read_b128 v[224:227], v249 offset:49152\nv_mfma_f32_16x16x32_bf16 v[164:167], v[208:211], v[132:135], v[164:167]\n" \
  "s_waitcnt lgkmcnt(3)\nv_mfma_f32_16x16x32_bf16 v[168:171], v[212:215], v[60:63], v[168:171]\nds_read_b128 v[208:211], v249 offset:50176\nv_mfma_f32_16x16x32_bf16 v[172:175], v[212:215], v[132:135], v[172:175]\ns_waitcnt vmcnt(14)\ns_barrier\n" \
  "s_waitcnt lgkmcnt(3)\nv_mfma_f32_16x16x32_bf16 v[160:163], v[216:219], v[64:67], v[160:163]\nds_read_b128 v[212:215], v249 offset:51200\nv_mfma_f32_16x16x32_bf16 v[164:167], v[216:219], v[136:139], v[164:167]\ns_add_u32 m0, s40, 65536\ns_nop 0\n" \
  "buffer_load_dwordx4 v251, %[rw], s43 offen sc1 lds\ns_waitcnt lgkmcnt(3)\nv_mfma_f32_16x16x32_bf16 v[168:171], v[220:223], v[64:67], v[168:171]\nds_read_b128 v[216:219], v249 offset:52224\nv_mfma_f32_16x16x32_bf16 v[172:175], v[220:223], v[136:139], v[172:175]\ns_waitcnt lgkmcnt(3)\n" \
  "v_mfma_f32_16x16x32_bf16 v[160:163], v[224:227], v[68:71], v[160:163]\nds_read_b128 v[220:223], v249 offset:53248\nv_mfma_f32_16x16x32_bf16 v[164:167], v[224:227], v[140:143], v[164:167]\ns_add_u32 m0, s40, 73728\ns_add_u32 s49, s42, 335872\nbuffer_load_dwordx4 v251, %[rw], s49 offen sc1 lds\n" \
  "s_waitcnt lgkmcnt(3)\nv_mfma_f32_16x16x32_bf16 v[168:171], v[208:211], v[68:71], v[168:171]\nds_read_b128 v[224:227], v249 offset:54272\nv_mfma_f32_16x16x32_bf16 v[172:175], v[208:211], v[140:143], v[172:175]\ns_waitcnt lgkmcnt(3)\nv_mfma_f32_16x16x32_bf16 v[176:179], v[212:215], v[0:3], 0\n" \
  "ds_read_b128 v[208:211], v250 offset:0\nv_mfma_f32_16x16x32_bf16 v[180:183], v[212:215], v[72:75], 0\ns_add_u32 m0, s40, 81920\ns_add_u32 s49, s42, 344064\nbuffer_load_dwordx4 v251, %[rw], s49 offen sc1 lds\ns_waitcnt lgkmcnt(3)\n" \
  "v_mfma_f32_16x16x32_bf16 v[184:187], v[216:219], v[0:3], 0\nds_read_b128 v[212:215], v250 offset:1024\nv_mfma_f32_16x16x32_bf16 v[188:191], v[216:219], v[72:75], 0\ns_waitcnt lgkmcnt(3)\nv_mfma_f32_16x16x32_bf16 v[176:179], v[220:223], v[4:7], v[176:179]\nds_read_b128 v[216:219], v250 offset:2048\n" \
  "v_mfma_f32_16x16x32_bf16 v[180:183], v[220:223], v[76:79], v[180:183]\ns_add_u32 m0, s40, 90112\ns_add_u32 s49, s42, 352256\nbuffer_load_dwordx4 v251, %[rw], s49 offen sc1 lds\ns_waitcnt lgkmcnt(3)\nv_mfma_f32_16x16x32_bf16 v[184:187], v[224:227], v[4:7], v[184:187]\n" \
  "ds_read_b128 v[220:223], v250 offset:3072\nv_max_f32 v160, 0, v160\nv_mfma_f32_16x16x32_bf16 v[188:191], v[224:227], v[76:79], v[188:191]\nv_max_f32 v161, 0, v161\ns_waitcnt lgkmcnt(3)\nv_mfma_f32_16x16x32_bf16 v[176:179], v[208:211], v[8:11], v[176:179]\n" \
  "ds_read_b128 v[224:227], v250 offset:4096\nv_max_f32 v162, 0, v162\nv_mfma_f32_16x16x32_bf16 v[180:183], v[208:211], v[80:83], v[180:183]\nv_max_f32 v163, 0, v163\ns_waitcnt lgkmcnt(3)\nv_mfma_f32_16x16x32_bf16 v[184:187], v[212:215], v[8:11], v[184:187]\n" \
  "ds_read_b128 v[208:211], v250 offset:5120\nv_mul_f32 v232, v160, v168\nv_mfma_f32_16x16x32_bf16 v[188:191], v[212:215], v[80:83], v[188:191]\nv_mul_f32 v233, v161, v169\ns_waitcnt lgkmcnt(3)\nv_mfma_f32_16x16x32_bf16 v[176:179], v[216:219], v[12:15], v[176:179]\n" \
  "ds_read_b128 v[212:215], v250 offset:6144\nv_mul_f32 v234, v162, v170\nv_mfma_f32_16x16x32_bf16 v[180:183], v[216:219], v[84:87], v[180:183]\nv_mul_f32 v235, v163, v171\ns_waitcnt lgkmcnt(3)\nv_mfma_f32_16x16x32_bf16 v[184:187], v[220:223], v[12:15], v[184:187]\n" \
  "ds_read_b128 v[216:219], v250 offset:7168\nv_max_f32 v164, 0, v164\nv_mfma_f32_16x16x32_bf16 v[188:191], v[220:223], v[84:87], v[188:191]\nv_max_f32 v165, 0, v165\ns_waitcnt lgkmcnt(3)\nv_mfma_f32_16x16x32_bf16 v[176:179], v[224:227], v[16:19], v[176:179]\n" \
  "ds_read_b128 v[220:223], v250 offset:8192\nv_max_f32 v166, 0, v166\nv_mfma_f32_16x16x32_bf16 v[180:183], v[224:227], v[88:91], v[180:183]\nv_max_f32 v167, 0, v167\ns_waitcnt lgkmcnt(3)\nv_mfma_f32_16x16x32_bf16 v[184:187], v[208:211], v[16:19], v[184:187]\n" \
  "ds_read_b128 v[224:227], v250 offset:9216\nv_mul_f32 v236, v164, v172\nv_mfma_f32_16x16x32_bf16 v[188:191], v[208:211], v[88:91], v[188:191]\nv_mul_f32 v237, v165, v173\ns_waitcnt lgkmcnt(3)\nv_mfma_f32_16x16x32_bf16 v[176:179], v[212:215], v[20:23], v[176:179]\n" \
  "ds_read_b128 v[208:211], v250 offset:10240\nv_mul_f32 v238, v166, v174\nv_mfma_f32_16x16x32_bf16 v[180:183], v[212:215], v[92:95], v[180:183]\nv_mul_f32 v239, v167, v175\ns_waitcnt lgkmcnt(3)\nv_mfma_f32_16x16x32_bf16 v[184:187], v[216:219], v[20:23], v[184:187]\n" \
  "ds_read_b128 v[212:215], v250 offset:11264\nv_mfma_f32_16x16x32_bf16 v[188:191], v[216:219], v[92:95], v[188:191]\ns_waitcnt lgkmcnt(3)\nv_mfma_f32_16x16x32_bf16 v[176:179], v[220:223], v[24:27], v[176:179]\nds_read_b128 v[216:219], v250 offset:12288\nv_mfma_f32_16x16x32_bf16 v[180:183], v[220:223], v[96:99], v[180:183]\n" \
  "s_waitcnt lgkmcnt(3)\nv_mfma_f32_16x16x32_bf16 v[184:187], v[224:227], v[24:27], v[184:187]\nds_read_b128 v[220:223], v250 offset:13312\nv_mfma_f32_16x16x32_bf16 v[188:191], v[224:227], v[96:99], v[188:191]\ns_waitcnt vmcnt(5)\ns_mov_b64 exec, 1\n" \
  "global_atomic_add v230, v229, %[cout] offset:128\ns_mov_b64 exec, -1\ns_waitcnt lgkmcnt(3)\nv_mfma_f32_16x16x32_bf16 v[176:179], v[208:211], v[28:31], v[176:179]\nds_read_b128 v[224:227], v250 offset:14336\nv_mfma_f32_16x16x32_bf16 v[180:183], v[208:211], v[100:103], v[180:183]\n" \
  "s_waitcnt lgkmcnt(3)\nv_mfma_f32_16x16x32_bf16 v[184:187], v[212:215], v[28:31], v[184:187]\nds_read_b128 v[208:211], v250 offset:15360\nv_mfma_f32_16x16x32_bf16 v[188:191], v[212:215], v[100:103], v[188:191]\ns_waitcnt lgkmcnt(3)\nv_mfma_f32_16x16x32_bf16 v[176:179], v[216:219], v[32:35], v[176:179]\n" \
  "ds_read_b128 v[212:215], v250 offset:16384\nv_mfma_f32_16x16x32_bf16 v[180:183], v[216:219], v[104:107], v[180:183]\nglobal_load_dword v228, v230, %[cin] offset:0 sc1\ns_waitcnt lgkmcnt(3)\nv_mfma_f32_16x16x32_bf16 v[184:187], v[220:223], v[32:35], v[184:187]\nds_read_b128 v[216:219], v250 offset:17408\n" \
  "v_mfma_f32_16x16x32_bf16 v[188:191], v[220:223], v[104:107], v[188:191]\ns_waitcnt lgkmcnt(3)\nv_mfma_f32_16x16x32_bf16 v[176:179], v[224:227], v[36:39], v[176:179]\nds_read_b128 v[220:223], v250 offset:18432\nv_mfma_f32_16x16x32_bf16 v[180:183], v[224:227], v[108:111], v[180:183]\ns_waitcnt lgkmcnt(3)\n" \
  "v_mfma_f32_16x16x32_bf16 v[184:187], v[208:211], v[36:39], v[184:187]\nds_read_b128 v[224:227], v250 offset:19456\nv_mfma_f32_16x16x32_bf16 v[188:191], v[208:211], v[108:111], v[188:191]\ns_waitcnt lgkmcnt(3)\nv_mfma_f32_16x16x32_bf16 v[176:179], v[212:215], v[40:43], v[176:179]\nds_read_b128 v[208:211], v250 offset:20480\n" \
  "v_mfma_f32_16x16x32_bf16 v[180:183], v[212:215], v[112:115], v[180:183]\ns_waitcnt lgkmcnt(3)\nv_mfma_f32_16x16x32_bf16 v[184:187], v[216:219], v[40:43], v[184:187]\nds_read_b128 v[212:215], v250 offset:21504\nv_mfma_f32_16x16x32_bf16 v[188:191], v[216:219], v[112:115], v[188:191]\ns_waitcnt lgkmcnt(3)\n" \
  "v_mfma_f32_16x16x32_bf16 v[176:179], v[220:223], v[44:47], v[176:179]\nds_read_b128 v[216:219], v250 offset:22528\nv_mfma_f32_16x16x32_bf16 v[180:183], v[220:223], v[116:119], v[180:183]\ns_waitcnt lgkmcnt(3)\nv_mfma_f32_16x16x32_bf16 v[184:187], v[224:227], v[44:47], v[184:187]\nds_read_b128 v[220:223], v250 offset:23552\n" \
  "v_mfma_f32_16x16x32_bf16 v[188:191], v[224:227], v[116:119], v[188:191]\ns_waitcnt lgkmcnt(3)\nv_mfma_f32_16x16x32_bf16 v[176:179], v[208:211], v[48:51], v[176:179]\nds_read_b128 v[224:227], v250 offset:24576\nv_mfma_f32_16x16x32_bf16 v[180:183], v[208:211], v[120:123], v[180:183]\ns_waitcnt lgkmcnt(3)\n" \
  "v_mfma_f32_16x16x32_bf16 v[184:187], v[212:215], v[48:51], v[184:187]\nds_read_b128 v[208:211], v250 offset:25600\nv_mfma_f32_16x16x32_bf16 v[188:191], v[212:215], v[120:123], v[188:191]\ns_waitcnt lgkmcnt(3)\nv_mfma_f32_16x16x32_bf16 v[176:179], v[216:219], v[52:55], v[176:179]\nds_read_b128 v[212:215], v250 offset:26624\n" \
  "v_mfma_f32_16x16x32_bf16 v[180:183], v[216:219], v[124:127], v[180:183]\ns_waitcnt lgkmcnt(3)\nv_mfma_f32_16x16x32_bf16 v[184:187], v[220:223], v[52:55], v[184:187]\nds_read_b128 v[216:219], v250 offset:27648\nv_mfma_f32_16x16x32_bf16 v[188:191], v[220:223], v[124:127], v[188:191]\ns_waitcnt lgkmcnt(3)\n" \
  "v_mfma_f32_16x16x32_bf16 v[176:179], v[224:227], v[56:59], v[176:179]\nds_read_b128 v[220:223], v250 offset:28672\nv_mfma_f32_16x16x32_bf16 v[180:183], v[224:227], v[128:131], v[180:183]\ns_waitcnt lgkmcnt(3)\nv_mfma_f32_16x16x32_bf16 v[184:187], v[208:211], v[56:59], v[184:187]\nds_read_b128 v[224:227], v250 offset:29696\n" \
  "v_mfma_f32_16x16x32_bf16 v[188:191], v[208:211], v[128:131], v[188:191]\ns_waitcnt lgkmcnt(3)\nv_mfma_f32_16x16x32_bf16 v[176:179], v[212:215], v[60:63], v[176:179]\nds_read_b128 v[208:211], v250 offset:30720\nv_mfma_f32_16x16x32_bf16 v[180:183], v[212:215], v[132:135], v[180:183]\ns_waitcnt lgkmcnt(3)\n" \
  "v_mfma_f32_16x16x32_bf16 v[184:187], v[216:219], v[60:63], v[184:187]\nds_read_b128 v[212:215], v250 offset:31744\nv_mfma_f32_16x16x32_bf16 v[188:191], v[216:219], v[132:135], v[188:191]\ns_waitcnt vmcnt(0)\ns_mov_b32 s50, 0x400000\n10:\n" \
  "v_readfirstlane_b32 s49, v228\ns_sub_u32 s50, s50, 1\ns_cbranch_scc1 11f\ns_cmp_ge_u32 s49, 32\ns_cbranch_scc1 11f\ns_sleep 2\n" \
  "global_load_dword v228, v230, %[cin] offset:0 sc1\ns_waitcnt vmcnt(0)\ns_branch 10b\n11:\ns_barrier\ns_waitcnt lgkmcnt(3)\n" \
  "v_mfma_f32_16x16x32_bf16 v[176:179], v[220:223], v[64:67], v[176:179]\nds_read_b128 v[216:219], v250 offset:32768\nv_mfma_f32_16x16x32_bf16 v[180:183], v[220:223], v[136:139], v[180:183]\ns_add_u32 m0, s40, 98304\ns_add_u32 s49, s42, 360448\nbuffer_load_dwordx4 v251, %[rw], s49 offen sc1 lds\n" \
  "s_waitcnt lgkmcnt(3)\nv_mfma_f32_16x16x32_bf16 v[184:187], v[224:227], v[64:67], v[184:187]\nds_read_b128 v[220:223], v250 offset:33792\nv_mfma_f32_16x16x32_bf16 v[188:191], v[224:227], v[136:139], v[188:191]\ns_waitcnt lgkmcnt(3)\nv_mfma_f32_16x16x32_bf16 v[176:179], v[208:211], v[68:71], v[176:179]\n" \
  "ds_read_b128 v[224:227], v250 offset:34816\nv_mfma_f32_16x16x32_bf16 v[180:183], v[208:211], v[140:143], v[180:183]\ns_add_u32 m0, s40, 106496\ns_add_u32 s49, s42, 368640\nbuffer_load_dwordx4 v251, %[rw], s49 offen sc1 lds\ns_waitcnt lgkmcnt(3)\n" \
  "v_mfma_f32_16x16x32_bf16 v[184:187], v[212:215], v[68:71], v[184:187]\nds_read_b128 v[208:211], v250 offset:35840\nv_mfma_f32_16x16x32_bf16 v[188:191], v[212:215], v[140:143], v[188:191]\ns_waitcnt lgkmcnt(3)\nv_mfma_f32_16x16x32_bf16 v[192:195], v[216:219], v[0:3], 0\nds_read_b128 v[212:215], v248 offset:0\n" \
  "v_mfma_f32_16x16x32_bf16 v[196:199], v[216:219], v[72:75], 0\ns_add_u32 m0, s40, 114688\ns_add_u32 s49, s42, 376832\nbuffer_load_dwordx4 v251, %[rw], s49 offen sc1 lds\ns_waitcnt lgkmcnt(3)\nv_mfma_f32_16x16x32_bf16 v[200:203], v[220:223], v[0:3], 0\n" \
  "ds_read_b128 v[216:219], v248 offset:1024\nv_mfma_f32_16x16x32_bf16 v[204:207], v[220:223], v[72:75], 0\ns_waitcnt lgkmcnt(3)\nv_mfma_f32_16x16x32_bf16 v[144:147], v[224:227], v[0:3], 0\nds_read_b128 v[220:223], v248 offset:2048\nv_mfma_f32_16x16x32_bf16 v[148:151], v[224:227], v[72:75], 0\n" \
  "s_add_u32 m0, s40, 122880\ns_add_u32 s49, s42, 385024\nbuffer_load_dwordx4 v251, %[rw], s49 offen sc1 lds\ns_waitcnt lgkmcnt(3)\nv_mfma_f32_16x16x32_bf16 v[152:155], v[208:211], v[0:3], 0\nds_read_b128 v[224:227], v248 offset:3072\n" \
  "v_max_f32 v176, 0, v176\nv_mfma_f32_16x16x32_bf16 v[156:159], v[208:211], v[72:75], 0\nv_max_f32 v177, 0, v177\ns_waitcnt lgkmcnt(3)\nv_mfma_f32_16x16x32_bf16 v[192:195], v[212:215], v[4:7], v[192:195]\nds_read_b128 v[208:211], v248 offset:4096\n" \
  "v_max_f32 v178, 0, v178\nv_mfma_f32_16x16x32_bf16 v[196:199], v[212:215], v[76:79], v[196:199]\nbuffer_load_dwordx4 v[0:3], v251, %[rh], s45 offen offset:0 sc1\nv_max_f32 v179, 0, v179\ns_waitcnt lgkmcnt(3)\nv_mfma_f32_16x16x32_bf16 v[200:203], v[216:219], v[4:7], v[200:203]\n" \
  "ds_read_b128 v[212:215], v248 offset:5120\nv_mul_f32 v176, v176, v184\nv_mfma_f32_16x16x32_bf16 v[204:207], v[216:219], v[76:79], v[204:207]\nv_mul_f32 v177, v177, v185\ns_waitcnt lgkmcnt(3)\nv_mfma_f32_16x16x32_bf16 v[144:147], v[220:223], v[4:7], v[144:147]\n" \
  "ds_read_b128 v[216:219], v248 offset:6144\nv_mul_f32 v178, v178, v186\nv_mfma_f32_16x16x32_bf16 v[148:151], v[220:223], v[76:79], v[148:151]\nbuffer_load_dwordx4 v[72:75], v251, %[rh], s51 offen offset:0 sc1\nv_mul_f32 v179, v179, v187\ns_waitcnt lgkmcnt(3)\n" \
  "v_mfma_f32_16x16x32_bf16 v[152:155], v[224:227], v[4:7], v[152:155]\nds_read_b128 v[220:223], v248 offset:7168\nv_cvt_pk_bf16_f32 v240, v232, v176\nv_mfma_f32_16x16x32_bf16 v[156:159], v[224:227], v[76:79], v[156:159]\nv_cvt_pk_bf16_f32 v241, v233, v177\ns_waitcnt lgkmcnt(3)\n" \
  "v_mfma_f32_16x16x32_bf16 v[192:195], v[208:211], v[8:11], v[192:195]\nds_read_b128 v[224:227], v248 offset:8192\nv_cvt_pk_bf16_f32 v242, v234, v178\nv_mfma_f32_16x16x32_bf16 v[196:199], v[208:211], v[80:83], v[196:199]\nbuffer_load_dwordx4 v[4:7], v251, %[rh], s45 offen offset:2048 sc1\nv_cvt_pk_bf16_f32 v243, v235, v179\n" \
  "s_waitcnt lgkmcnt(3)\nv_mfma_f32_16x16x32_bf16 v[200:203], v[212:215], v[8:11], v[200:203]\nds_read_b128 v[208:211], v248 offset:9216\ns_add_u32 s49, s47, 20480\nbuffer_store_dwordx4 v[240:243], v251, %[rh], s49 offen sc1\nv_mfma_f32_16x16x32_bf16 v[204:207], v[212:215], v[80:83], v[204:207]\n" \
  "v_max_f32 v180, 0, v180\ns_waitcnt lgkmcnt(3)\nv_mfma_f32_16x16x32_bf16 v[144:147], v[216:219], v[8:11], v[144:147]\nds_read_b128 v[212:215], v248 offset:10240\nv_max_f32 v181, 0, v181\nv_mfma_f32_16x16x32_bf16 v[148:151], v[216:219], v[80:83], v[148:151]\n" \
  "buffer_load_dwordx4 v[76:79], v251, %[rh], s51 offen offset:2048 sc1\nv_max_f32 v182, 0, v182\ns_waitcnt lgkmcnt(3)\nv_mfma_f32_16x16x32_bf16 v[152:155], v[220:223], v[8:11], v[152:155]\nds_read_b128 v[216:219], v248 offset:11264\nv_max_f32 v183, 0, v183\n" \
  "v_mfma_f32_16x16x32_bf16 v[156:159], v[220:223], v[80:83], v[156:159]\nv_mul_f32 v180, v180, v188\ns_waitcnt lgkmcnt(3)\nv_mfma_f32_16x16x32_bf16 v[192:195], v[224:227], v[12:15], v[192:195]\nds_read_b128 v[220:223], v248 offset:12288\nv_mul_f32 v181, v181, v189\n" \
  "v_mfma_f32_16x16x32_bf16 v[196:199], v[224:227], v[84:87], v[196:199]\ns_add_u32 s49, s45, 4096\nbuffer_load_dwordx4 v[8:11], v251, %[rh], s49 offen offset:0 sc1\nv_mul_f32 v182, v182, v190\ns_waitcnt lgkmcnt(3)\nv_mfma_f32_16x16x32_bf16 v[200:203], v[208:211], v[12:15], v[200:203]\n" \
  "ds_read_b128 v[224:227], v248 offset:13312\nv_mul_f32 v183, v183, v191\nv_mfma_f32_16x16x32_bf16 v[204:207], v[208:211], v[84:87], v[204:207]\nv_cvt_pk_bf16_f32 v244, v236, v180\ns_waitcnt lgkmcnt(3)\nv_mfma_f32_16x16x32_bf16 v[144:147], v[212:215], v[12:15], v[144:147]\n" \
  "ds_read_b128 v[208:211], v248 offset:14336\nv_cvt_pk_bf16_f32 v245, v237, v181\nv_mfma_f32_16x16x32_bf16 v[148:151], v[212:215], v[84:87], v[148:151]\ns_add_u32 s49, s51, 4096\nbuffer_load_dwordx4 v[80:83], v251, %[rh], s49 offen offset:0 sc1\nv_cvt_pk_bf16_f32 v246, v238, v182\n" \
  "s_waitcnt lgkmcnt(3)\nv_mfma_f32_16x16x32_bf16 v[152:155], v[216:219], v[12:15], v[152:155]\nds_read_b128 v[212:215], v248 offset:15360\nv_cvt_pk_bf16_f32 v247, v239, v183\nv_mfma_f32_16x16x32_bf16 v[156:159], v[216:219], v[84:87], v[156:159]\ns_add_u32 s49, s47, 167936\n" \
  "buffer_store_dwordx4 v[244:247], v251, %[rh], s49 offen sc1\ns_waitcnt lgkmcnt(3)\nv_mfma_f32_16x16x32_bf16 v[192:195], v[220:223], v[16:19], v[192:195]\nds_read_b128 v[216:219], v248 offset:16384\nv_mfma_f32_16x16x32_bf16 v[196:199], v[220:223], v[88:91], v[196:199]\ns_add_u32 s49, s45, 4096\n" \
  "buffer_load_dwordx4 v[12:15], v251, %[rh], s49 offen offset:2048 sc1\ns_waitcnt lgkmcnt(3)\nv_mfma_f32_16x16x32_bf16 v[200:203], v[224:227], v[16:19], v[200:203]\nds_read_b128 v[220:223], v248 offset:17408\nv_mfma_f32_16x16x32_bf16 v[204:207], v[224:227], v[88:91], v[204:207]\ns_waitcnt lgkmcnt(3)\n" \
  "v_mfma_f32_16x16x32_bf16 v[144:147], v[208:211], v[16:19], v[144:147]\nds_read_b128 v[224:227], v248 offset:18432\nv_mfma_f32_16x16x32_bf16 v[148:151], v[208:211], v[88:91], v[148:151]\ns_add_u32 s49, s51, 4096\nbuffer_load_dwordx4 v[84:87], v251, %[rh], s49 offen offset:2048 sc1\ns_waitcnt lgkmcnt(3)\n" \
  "v_mfma_f32_16x16x32_bf16 v[152:155], v[212:215], v[16:19], v[152:155]\nds_read_b128 v[208:211], v248 offset:19456\nv_mfma_f32_16x16x32_bf16 v[156:159], v[212:215], v[88:91], v[156:159]\ns_waitcnt lgkmcnt(3)\nv_mfma_f32_16x16x32_bf16 v[192:195], v[216:219], v[20:23], v[192:195]\nds_read_b128 v[212:215], v248 offset:20480\n" \
  "v_mfma_f32_16x16x32_bf16 v[196:199], v[216:219], v[92:95], v[196:199]\ns_add_u32 m0, s40, 131072\ns_add_u32 s49, s42, 393216\nbuffer_load_dwordx4 v251, %[rw], s49 offen sc1 lds\ns_waitcnt lgkmcnt(3)\nv_mfma_f32_16x16x32_bf16 v[200:203], v[220:223], v[20:23], v[200:203]\n" \
  "ds_read_b128 v[216:219], v248 offset:21504\nv_mfma_f32_16x16x32_bf16 v[204:207], v[220:223], v[92:95], v[204:207]\ns_waitcnt lgkmcnt(3)\nv_mfma_f32_16x16x32_bf16 v[144:147], v[224:227], v[20:23], v[144:147]\nds_read_b128 v[220:223], v248 offset:22528\nv_mfma_f32_16x16x32_bf16 v[148:151], v[224:227], v[92:95], v[148:151]\n" \
  "s_add_u32 s49, s45, 8192\nbuffer_load_dwordx4 v[16:19], v251, %[rh], s49 offen offset:0 sc1\ns_waitcnt lgkmcnt(3)\nv_mfma_f32_16x16x32_bf16 v[152:155], v[208:211], v[20:23], v[152:155]\nds_read_b128 v[224:227], v248 offset:23552\nv_mfma_f32_16x16x32_bf16 v[156:159], v[208:211], v[92:95], v[156:159]\n" \
  "s_waitcnt lgkmcnt(3)\nv_mfma_f32_16x16x32_bf16 v[192:195], v[212:215], v[24:27], v[192:195]\nds_read_b128 v[208:211], v248 offset:24576\nv_mfma_f32_16x16x32_bf16 v[196:199], v[212:215], v[96:99], v[196:199]\ns_add_u32 s49, s51, 8192\nbuffer_load_dwordx4 v[88:91], v251, %[rh], s49 offen offset:0 sc1\n" \
  "s_waitcnt lgkmcnt(3)\nv_mfma_f32_16x16x32_bf16 v[200:203], v[216:219], v[24:27], v[200:203]\nds_read_b128 v[212:215], v248 offset:25600\nv_mfma_f32_16x16x32_bf16 v[204:207], v[216:219], v[96:99], v[204:207]\ns_waitcnt lgkmcnt(3)\nv_mfma_f32_16x16x32_bf16 v[144:147], v[220:223], v[24:27], v[144:147]\n" \
  "ds_read_b128 v[216:219], v248 offset:26624\nv_mfma_f32_16x16x32_bf16 v[148:151], v[220:223], v[96:99], v[148:151]\ns_add_u32 s49, s45, 8192\nbuffer_load_dwordx4 v[20:23], v251, %[rh], s49 offen offset:2048 sc1\ns_waitcnt lgkmcnt(3)\nv_mfma_f32_16x16x32_bf16 v[152:155], v[224:227], v[24:27], v[152:155]\n" \
  "ds_read_b128 v[220:223], v248 offset:27648\nv_mfma_f32_16x16x32_bf16 v[156:159], v[224:227], v[96:99], v[156:159]\ns_waitcnt vmcnt(28)\ns_barrier\ns_waitcnt lgkmcnt(3)\nv_mfma_f32_16x16x32_bf16 v[192:195], v[208:211], v[28:31], v[192:195]\n" \
  "ds_read_b128 v[224:227], v248 offset:28672\nv_mfma_f32_16x16x32_bf16 v[196:199], v[208:211], v[100:103], v[196:199]\ns_add_u32 m0, s40, 139264\ns_add_u32 s49, s42, 401408\nbuffer_load_dwordx4 v251, %[rw], s49 offen sc1 lds\ns_waitcnt lgkmcnt(3)\n" \
  "v_mfma_f32_16x16x32_bf16 v[200:203], v[212:215], v[28:31], v[200:203]\nds_read_b128 v[208:211], v248 offset:29696\nv_mfma_f32_16x16x32_bf16 v[204:207], v[212:215], v[100:103], v[204:207]\ns_waitcnt lgkmcnt(3)\nv_mfma_f32_16x16x32_bf16 v[144:147], v[216:219], v[28:31], v[144:147]\nds_read_b128 v[212:215], v248 offset:30720\n" \
  "v_mfma_f32_16x16x32_bf16 v[148:151], v[216:219], v[100:103], v[148:151]\ns_add_u32 m0, s40, 0\ns_add_u32 s49, s42, 409600\nbuffer_load_dwordx4 v251, %[rw], s49 offen sc1 lds\ns_waitcnt lgkmcnt(3)\nv_mfma_f32_16x16x32_bf16 v[152:155], v[220:223], v[28:31], v[152:155]\n" \
  "ds_read_b128 v[216:219], v248 offset:31744\nv_mfma_f32_16x16x32_bf16 v[156:159], v[220:223], v[100:103], v[156:159]\ns_waitcnt lgkmcnt(3)\nv_mfma_f32_16x16x32_bf16 v[192:195], v[224:227], v[32:35], v[192:195]\nds_read_b128 v[220:223], v248 offset:32768\nv_mfma_f32_16x16x32_bf16 v[196:199], v[224:227], v[104:107], v[196:199]\n" \
  "s_add_u32 m0, s40, 8192\ns_add_u32 s49, s42, 417792\nbuffer_load_dwordx4 v251, %[rw], s49 offen sc1 lds\ns_waitcnt lgkmcnt(3)\nv_mfma_f32_16x16x32_bf16 v[200:203], v[208:211], v[32:35], v[200:203]\nds_read_b128 v[224:227], v248 offset:33792\n" \
  "v_mfma_f32_16x16x32_bf16 v[204:207], v[208:211], v[104:107], v[204:207]\ns_waitcnt lgkmcnt(3)\nv_mfma_f32_16x16x32_bf16 v[144:147], v[212:215], v[32:35], v[144:147]\nds_read_b128 v[208:211], v248 offset:34816\nv_mfma_f32_16x16x32_bf16 v[148:151], v[212:215], v[104:107], v[148:151]\ns_add_u32 m0, s40, 16384\n" \
  "s_add_u32 s49, s42, 425984\nbuffer_load_dwordx4 v251, %[rw], s49 offen sc1 lds\ns_waitcnt lgkmcnt(3)\nv_mfma_f32_16x16x32_bf16 v[152:155], v[216:219], v[32:35], v[152:155]\nds_read_b128 v[212:215], v248 offset:35840\nv_mfma_f32_16x16x32_bf16 v[156:159], v[216:219], v[104:107], v[156:159]\n" \
  "s_waitcnt lgkmcnt(3)\nv_mfma_f32_16x16x32_bf16 v[192:195], v[220:223], v[36:39], v[192:195]\nds_read_b128 v[216:219], v248 offset:36864\nv_mfma_f32_16x16x32_bf16 v[196:199], v[220:223], v[108:111], v[196:199]\ns_add_u32 s49, s51, 8192\nbuffer_load_dwordx4 v[92:95], v251, %[rh], s49 offen offset:2048 sc1\n" \
  "global_load_dword v228, v230, %[cin] offset:128 sc1\ns_waitcnt lgkmcnt(3)\nv_mfma_f32_16x16x32_bf16 v[200:203], v[224:227], v[36:39], v[200:203]\nds_read_b128 v[220:223], v248 offset:37888\nv_mfma_f32_16x16x32_bf16 v[204:207], v[224:227], v[108:111], v[204:207]\ns_waitcnt lgkmcnt(3)\n" \
  "v_mfma_f32_16x16x32_bf16 v[144:147], v[208:211], v[36:39], v[144:147]\nds_read_b128 v[224:227], v248 offset:38912\nv_mfma_f32_16x16x32_bf16 v[148:151], v[208:211], v[108:111], v[148:151]\ns_waitcnt lgkmcnt(3)\nv_mfma_f32_16x16x32_bf16 v[152:155], v[212:215], v[36:39], v[152:155]\nds_read_b128 v[208:211], v248 offset:39936\n" \
  "v_mfma_f32_16x16x32_bf16 v[156:159], v[212:215], v[108:111], v[156:159]\ns_waitcnt lgkmcnt(3)\nv_mfma_f32_16x16x32_bf16 v[192:195], v[216:219], v[40:43], v[192:195]\nds_read_b128 v[212:215], v248 offset:40960\nv_mfma_f32_16x16x32_bf16 v[196:199], v[216:219], v[112:115], v[196:199]\ns_waitcnt lgkmcnt(3)\n" \
  "v_mfma_f32_16x16x32_bf16 v[200:203], v[220:223], v[40:43], v[200:203]\nds_read_b128 v[216:219], v248 offset:41984\nv_mfma_f32_16x16x32_bf16 v[204:207], v[220:223], v[112:115], v[204:207]\ns_waitcnt lgkmcnt(3)\nv_mfma_f32_16x16x32_bf16 v[144:147], v[224:227], v[40:43], v[144:147]\nds_read_b128 v[220:223], v248 offset:43008\n" \
  "v_mfma_f32_16x16x32_bf16 v[148:151], v[224:227], v[112:115], v[148:151]\ns_waitcnt lgkmcnt(3)\nv_mfma_f32_16x16x32_bf16 v[152:155], v[208:211], v[40:43], v[152:155]\nds_read_b128 v[224:227], v248 offset:44032\nv_mfma_f32_16x16x32_bf16 v[156:159], v[208:211], v[112:115], v[156:159]\ns_waitcnt vmcnt(12)\n" \
  "s_mov_b64 exec, 1\nglobal_atomic_add v230, v229, %[cout] offset:256\ns_mov_b64 exec, -1\ns_waitcnt lgkmcnt(3)\nv_mfma_f32_16x16x32_bf16 v[192:195], v[212:215], v[44:47], v[192:195]\nds_read_b128 v[208:211], v248 offset:45056\n" \
  "v_mfma_f32_16x16x32_bf16 v[196:199], v[212:215], v[116:119], v[196:199]\ns_waitcnt lgkmcnt(3)\nv_mfma_f32_16x16x32_bf16 v[200:203], v[216:219], v[44:47], v[200:203]\nds_read_b128 v[212:215], v248 offset:46080\nv_mfma_f32_16x16x32_bf16 v[204:207], v[216:219], v[116:119], v[204:207]\ns_waitcnt lgkmcnt(3)\n" \
  "v_mfma_f32_16x16x32_bf16 v[144:147], v[220:223], v[44:47], v[144:147]\nds_read_b128 v[216:219], v248 offset:47104\nv_mfma_f32_16x16x32_bf16 v[148:151], v[220:223], v[116:119], v[148:151]\ns_waitcnt lgkmcnt(3)\nv_mfma_f32_16x16x32_bf16 v[152:155], v[224:227], v[44:47], v[152:155]\nds_read_b128 v[220:223], v248 offset:48128\n" \
  "v_mfma_f32_16x16x32_bf16 v[156:159], v[224:227], v[116:119], v[156:159]\ns_waitcnt lgkmcnt(3)\nv_mfma_f32_16x16x32_bf16 v[192:195], v[208:211], v[48:51], v[192:195]\nds_read_b128 v[224:227], v248 offset:49152\nv_mfma_f32_16x16x32_bf16 v[196:199], v[208:211], v[120:123], v[196:199]\ns_waitcnt lgkmcnt(3)\n" \
  "v_mfma_f32_16x16x32_bf16 v[200:203], v[212:215], v[48:51], v[200:203]\nds_read_b128 v[208:211], v248 offset:50176\nv_mfma_f32_16x16x32_bf16 v[204:207], v[212:215], v[120:123], v[204:207]\ns_waitcnt lgkmcnt(3)\nv_mfma_f32_16x16x32_bf16 v[144:147], v[216:219], v[48:51], v[144:147]\nds_read_b128 v[212:215], v248 offset:51200\n" \
  "v_mfma_f32_16x16x32_bf16 v[148:151], v[216:219], v[120:123], v[148:151]\ns_waitcnt lgkmcnt(3)\nv_mfma_f32_16x16x32_bf16 v[152:155], v[220:223], v[48:51], v[152:155]\nds_read_b128 v[216:219], v248 offset:52224\nv_mfma_f32_16x16x32_bf16 v[156:159], v[220:223], v[120:123], v[156:159]\ns_waitcnt vmcnt(1)\n" \
  "s_mov_b32 s50, 0x400000\n12:\nv_readfirstlane_b32 s49, v228\ns_sub_u32 s50, s50, 1\ns_cbranch_scc1 13f\ns_cmp_ge_u32 s49, 32\n" \
  "s_cbranch_scc1 13f\ns_sleep 2\nglobal_load_dword v228, v230, %[cin] offset:128 sc1\ns_waitcnt vmcnt(0)\ns_branch 12b\n13:\n" \
  "s_barrier\ns_waitcnt lgkmcnt(3)\nv_mfma_f32_16x16x32_bf16 v[192:195], v[224:227], v[52:55], v[192:195]\nds_read_b128 v[220:223], v248 offset:53248\nv_mfma_f32_16x16x32_bf16 v[196:199], v[224:227], v[124:127], v[196:199]\ns_add_u32 s49, s45, 12288\n" \
  "buffer_load_dwordx4 v[24:27], v251, %[rh], s49 offen offset:0 sc1\ns_waitcnt lgkmcnt(3)\nv_mfma_f32_16x16x32_bf16 v[200:203], v[208:211], v[52:55], v[200:203]\nds_read_b128 v[224:227], v248 offset:54272\nv_mfma_f32_16x16x32_bf16 v[204:207], v[208:211], v[124:127], v[204:207]\ns_waitcnt lgkmcnt(3)\n" \
  "v_mfma_f32_16x16x32_bf16 v[144:147], v[212:215], v[52:55], v[144:147]\nds_read_b128 v[208:211], v249 offset:0\nv_mfma_f32_16x16x32_bf16 v[148:151], v[212:215], v[124:127], v[148:151]\ns_add_u32 s49, s51, 12288\nbuffer_load_dwordx4 v[96:99], v251, %[rh], s49 offen offset:0 sc1\ns_waitcnt lgkmcnt(3)\n" \
  "v_mfma_f32_16x16x32_bf16 v[152:155], v[216:219], v[52:55], v[152:155]\nds_read_b128 v[212:215], v249 offset:1024\nv_mfma_f32_16x16x32_bf16 v[156:159], v[216:219], v[124:127], v[156:159]\ns_waitcnt lgkmcnt(3)\nv_mfma_f32_16x16x32_bf16 v[192:195], v[220:223], v[56:59], v[192:195]\nds_read_b128 v[216:219], v249 offset:2048\n" \
  "v_mfma_f32_16x16x32_bf16 v[196:199], v[220:223], v[128:131], v[196:199]\ns_add_u32 m0, s40, 24576\ns_add_u32 s49, s42, 434176\nbuffer_load_dwordx4 v251, %[rw], s49 offen sc1 lds\ns_waitcnt lgkmcnt(3)\nv_mfma_f32_16x16x32_bf16 v[200:203], v[224:227], v[56:59], v[200:203]\n" \
  "ds_read_b128 v[220:223], v249 offset:3072\nv_mfma_f32_16x16x32_bf16 v[204:207], v[224:227], v[128:131], v[204:207]\ns_waitcnt lgkmcnt(3)\nv_mfma_f32_16x16x32_bf16 v[144:147], v[208:211], v[56:59], v[144:147]\nds_read_b128 v[224:227], v249 offset:4096\nv_mfma_f32_16x16x32_bf16 v[148:151], v[208:211], v[128:131], v[148:151]\n" \
  "s_add_u32 m0, s40, 32768\ns_add_u32 s49, s42, 442368\nbuffer_load_dwordx4 v251, %[rw], s49 offen sc1 lds\ns_waitcnt lgkmcnt(3)\nv_mfma_f32_16x16x32_bf16 v[152:155], v[212:215], v[56:59], v[152:155]\nds_read_b128 v[208:211], v249 offset:5120\n" \
  "v_mfma_f32_16x16x32_bf16 v[156:159], v[212:215], v[128:131], v[156:159]\ns_waitcnt lgkmcnt(3)\nv_mfma_f32_16x16x32_bf16 v[192:195], v[216:219], v[60:63], v[192:195]\nds_read_b128 v[212:215], v249 offset:6144\nv_mfma_f32_16x16x32_bf16 v[196:199], v[216:219], v[132:135], v[196:199]\ns_add_u32 m0, s40, 40960\n" \
  "s_add_u32 s49, s42, 450560\nbuffer_load_dwordx4 v251, %[rw], s49 offen sc1 lds\ns_waitcnt lgkmcnt(3)\nv_mfma_f32_16x16x32_bf16 v[200:203], v[220:223], v[60:63], v[200:203]\nds_read_b128 v[216:219], v249 offset:7168\nv_mfma_f32_16x16x32_bf16 v[204:207], v[220:223], v[132:135], v[204:207]\n" \
  "s_waitcnt lgkmcnt(3)\nv_mfma_f32_16x16x32_bf16 v[192:195], v[224:227], v[64:67], v[192:195]\nds_read_b128 v[220:223], v249 offset:8192\nv_mfma_f32_16x16x32_bf16 v[196:199], v[224:227], v[136:139], v[196:199]\ns_add_u32 s49, s45, 12288\nbuffer_load_dwordx4 v[28:31], v251, %[rh], s49 offen offset:2048 sc1\n" \
  "s_waitcnt lgkmcnt(3)\nv_mfma_f32_16x16x32_bf16 v[200:203], v[208:211], v[64:67], v[200:203]\nds_read_b128 v[224:227], v249 offset:9216\nv_mfma_f32_16x16x32_bf16 v[204:207], v[208:211], v[136:139], v[204:207]\ns_waitcnt lgkmcnt(3)\nv_mfma_f32_16x16x32_bf16 v[192:195], v[212:215], v[68:71], v[192:195]\n" \
  "ds_read_b128 v[208:211], v249 offset:10240\nv_mfma_f32_16x16x32_bf16 v[196:199], v[212:215], v[140:143], v[196:199]\ns_add_u32 s49, s51, 12288\nbuffer_load_dwordx4 v[100:103], v251, %[rh], s49 offen offset:2048 sc1\ns_waitcnt lgkmcnt(3)\nv_mfma_f32_16x16x32_bf16 v[200:203], v[216:219], v[68:71], v[200:203]\n" \
  "ds_read_b128 v[212:215], v249 offset:11264\nv_mfma_f32_16x16x32_bf16 v[204:207], v[216:219], v[140:143], v[204:207]\ns_waitcnt lgkmcnt(3)\nv_mfma_f32_16x16x32_bf16 v[144:147], v[220:223], v[60:63], v[144:147]\nds_read_b128 v[216:219], v249 offset:12288\nv_mfma_f32_16x16x32_bf16 v[148:151], v[220:223], v[132:135], v[148:151]\n" \
  "s_add_u32 s49, s45, 16384\nbuffer_load_dwordx4 v[32:35], v251, %[rh], s49 offen offset:0 sc1\ns_waitcnt lgkmcnt(3)\nv_mfma_f32_16x16x32_bf16 v[152:155], v[224:227], v[60:63], v[152:155]\nds_read_b128 v[220:223], v249 offset:13312\nv_mfma_f32_16x16x32_bf16 v[156:159], v[224:227], v[132:135], v[156:159]\n" \
  "s_waitcnt lgkmcnt(3)\nv_mfma_f32_16x16x32_bf16 v[144:147], v[208:211], v[64:67], v[144:147]\nv_add_u32 v248, s48, v248\nv_add_u32 v249, s48, v249\nv_add_u32 v250, s48, v250\nds_read_b128 v[224:227], v249 offset:14336\n" \
  "v_mfma_f32_16x16x32_bf16 v[148:151], v[208:211], v[136:139], v[148:151]\ns_add_u32 s49, s51, 16384\nbuffer_load_dwordx4 v[104:107], v251, %[rh], s49 offen offset:0 sc1\ns_waitcnt lgkmcnt(3)\nv_mfma_f32_16x16x32_bf16 v[152:155], v[212:215], v[64:67], v[152:155]\nds_read_b128 v[208:211], v249 offset:16384\n" \
  "v_max_f32 v192, 0, v192\nv_mfma_f32_16x16x32_bf16 v[156:159], v[212:215], v[136:139], v[156:159]\nv_max_f32 v193, 0, v193\ns_waitcnt lgkmcnt(3)\nv_mfma_f32_16x16x32_bf16 v[144:147], v[216:219], v[68:71], v[144:147]\nds_read_b128 v[212:215], v249 offset:18432\n" \
  "v_max_f32 v194, 0, v194\nv_mfma_f32_16x16x32_bf16 v[148:151], v[216:219], v[140:143], v[148:151]\ns_add_u32 s49, s45, 16384\nbuffer_load_dwordx4 v[36:39], v251, %[rh], s49 offen offset:2048 sc1\nv_max_f32 v195, 0, v195\ns_waitcnt lgkmcnt(3)\n" \
  "v_mfma_f32_16x16x32_bf16 v[152:155], v[220:223], v[68:71], v[152:155]\nds_read_b128 v[216:219], v249 offset:20480\nv_mul_f32 v232, v192, v200\nv_mfma_f32_16x16x32_bf16 v[156:159], v[220:223], v[140:143], v[156:159]\nv_mul_f32 v233, v193, v201\ns_waitcnt vmcnt(29)\n" \
  "s_barrier\ns_waitcnt lgkmcnt(3)\nv_mfma_f32_16x16x32_bf16 v[160:163], v[224:227], v[0:3], 0\nds_read_b128 v[220:223], v249 offset:22528\nv_mul_f32 v234, v194, v202\nv_mfma_f32_16x16x32_bf16 v[164:167], v[224:227], v[72:75], 0\n" \
  "s_add_u32 m0, s40, 49152\ns_add_u32 s49, s42, 458752\nbuffer_load_dwordx4 v251, %[rw], s49 offen sc1 lds\nv_mul_f32 v235, v195, v203\nv_max_f32 v196, 0, v196\nv_max_f32 v197, 0, v197\n" \
  "s_waitcnt lgkmcnt(3)\nv_mfma_f32_16x16x32_bf16 v[168:171], v[208:211], v[0:3], 0\nds_read_b128 v[224:227], v249 offset:24576\nv_max_f32 v198, 0, v198\nv_mfma_f32_16x16x32_bf16 v[172:175], v[208:211], v[72:75], 0\nv_max_f32 v199, 0, v199\n" \
  "v_mul_f32 v236, v196, v204\nv_mul_f32 v237, v197, v205\ns_waitcnt lgkmcnt(3)\nv_mfma_f32_16x16x32_bf16 v[176:179], v[212:215], v[0:3], 0\nds_read_b128 v[208:211], v249 offset:26624\nv_mul_f32 v238, v198, v206\n" \
  "v_mfma_f32_16x16x32_bf16 v[180:183], v[212:215], v[72:75], 0\ns_add_u32 m0, s40, 57344\ns_add_u32 s49, s42, 466944\nbuffer_load_dwordx4 v251, %[rw], s49 offen sc1 lds\nv_mul_f32 v239, v199, v207\ns_waitcnt lgkmcnt(3)\n" \
  "v_mfma_f32_16x16x32_bf16 v[184:187], v[216:219], v[0:3], 0\nds_read_b128 v[212:215], v249 offset:28672\nv_max_f32 v144, 0, v144\nv_mfma_f32_16x16x32_bf16 v[188:191], v[216:219], v[72:75], 0\nv_max_f32 v145, 0, v145\nv_max_f32 v146, 0, v146\n" \
  "v_max_f32 v147, 0, v147\ns_waitcnt vmcnt(28)\ns_waitcnt lgkmcnt(3)\nv_mfma_f32_16x16x32_bf16 v[160:163], v[220:223], v[4:7], v[160:163]\nds_read_b128 v[216:219], v249 offset:30720\nv_mul_f32 v144, v144, v152\n" \
  "v_mfma_f32_16x16x32_bf16 v[164:167], v[220:223], v[76:79], v[164:167]\ns_add_u32 s49, s51, 16384\nbuffer_load_dwordx4 v[108:111], v251, %[rh], s49 offen offset:2048 sc1\nv_mul_f32 v145, v145, v153\nv_mul_f32 v146, v146, v154\nv_mul_f32 v147, v147, v155\n" \
  "s_waitcnt lgkmcnt(3)\nv_mfma_f32_16x16x32_bf16 v[168:171], v[224:227], v[4:7], v[168:171]\nds_read_b128 v[220:223], v249 offset:32768\nv_cvt_pk_bf16_f32 v240, v232, v144\nv_mfma_f32_16x16x32_bf16 v[172:175], v[224:227], v[76:79], v[172:175]\nv_cvt_pk_bf16_f32 v241, v233, v145\n" \
  "v_cvt_pk_bf16_f32 v242, v234, v146\nv_cvt_pk_bf16_f32 v243, v235, v147\ns_waitcnt lgkmcnt(3)\nv_mfma_f32_16x16x32_bf16 v[176:179], v[208:211], v[4:7], v[176:179]\nds_read_b128 v[224:227], v249 offset:34816\ns_add_u32 s49, s47, 28672\n" \
  "buffer_store_dwordx4 v[240:243], v251, %[rh], s49 offen sc1\nv_mfma_f32_16x16x32_bf16 v[180:183], v[208:211], v[76:79], v[180:183]\nv_max_f32 v148, 0, v148\nv_max_f32 v149, 0, v149\nv_max_f32 v150, 0, v150\ns_waitcnt lgkmcnt(3)\n" \
  "v_mfma_f32_16x16x32_bf16 v[184:187], v[212:215], v[4:7], v[184:187]\nds_read_b128 v[208:211], v249 offset:36864\nv_max_f32 v151, 0, v151\nv_mfma_f32_16x16x32_bf16 v[188:191], v[212:215], v[76:79], v[188:191]\nv_mul_f32 v148, v148, v156\nv_mul_f32 v149, v149, v157\n" \
  "v_mul_f32 v150, v150, v158\ns_waitcnt vmcnt(28)\ns_waitcnt lgkmcnt(3)\nv_mfma_f32_16x16x32_bf16 v[160:163], v[216:219], v[8:11], v[160:163]\nds_read_b128 v[212:215], v249 offset:38912\nv_mul_f32 v151, v151, v159\n" \
  "v_mfma_f32_16x16x32_bf16 v[164:167], v[216:219], v[80:83], v[164:167]\nv_cvt_pk_bf16_f32 v244, v236, v148\nv_cvt_pk_bf16_f32 v245, v237, v149\nv_cvt_pk_bf16_f32 v246, v238, v150\ns_waitcnt lgkmcnt(3)\nv_mfma_f32_16x16x32_bf16 v[168:171], v[220:223], v[8:11], v[168:171]\n" \
  "ds_read_b128 v[216:219], v249 offset:40960\nv_cvt_pk_bf16_f32 v247, v239, v151\nv_mfma_f32_16x16x32_bf16 v[172:175], v[220:223], v[80:83], v[172:175]\ns_add_u32 s49, s47, 176128\nbuffer_store_dwordx4 v[244:247], v251, %[rh], s49 offen sc1\ns_waitcnt lgkmcnt(3)\n" \
  "v_mfma_f32_16x16x32_bf16 v[176:179], v[224:227], v[8:11], v[176:179]\nds_read_b128 v[220:223], v249 offset:43008\nv_mfma_f32_16x16x32_bf16 v[180:183], v[224:227], v[80:83], v[180:183]\ns_waitcnt lgkmcnt(3)\nv_mfma_f32_16x16x32_bf16 v[184:187], v[208:211], v[8:11], v[184:187]\nds_read_b128 v[224:227], v249 offset:45056\n" \
  "v_mfma_f32_16x16x32_bf16 v[188:191], v[208:211], v[80:83], v[188:191]\ns_waitcnt lgkmcnt(3)\nv_mfma_f32_16x16x32_bf16 v[192:195], v[212:215], v[0:3], 0\nds_read_b128 v[208:211], v249 offset:47104\nv_mfma_f32_16x16x32_bf16 v[196:199], v[212:215], v[72:75], 0\ns_waitcnt lgkmcnt(3)\n" \
  "v_mfma_f32_16x16x32_bf16 v[200:203], v[216:219], v[0:3], 0\nds_read_b128 v[212:215], v249 offset:49152\nv_mfma_f32_16x16x32_bf16 v[204:207], v[216:219], v[72:75], 0\ns_waitcnt lgkmcnt(3)\nv_mfma_f32_16x16x32_bf16 v[192:195], v[220:223], v[4:7], v[192:195]\nds_read_b128 v[216:219], v249 offset:51200\n" \
  "v_mfma_f32_16x16x32_bf16 v[196:199], v[220:223], v[76:79], v[196:199]\ns_waitcnt lgkmcnt(3)\nv_mfma_f32_16x16x32_bf16 v[200:203], v[224:227], v[4:7], v[200:203]\nds_read_b128 v[220:223], v249 offset:53248\nv_mfma_f32_16x16x32_bf16 v[204:207], v[224:227], v[76:79], v[204:207]\ns_waitcnt lgkmcnt(3)\n" \
  "v_mfma_f32_16x16x32_bf16 v[192:195], v[208:211], v[8:11], v[192:195]\nds_read_b128 v[224:227], v250 offset:0\nv_mfma_f32_16x16x32_bf16 v[196:199], v[208:211], v[80:83], v[196:199]\ns_waitcnt lgkmcnt(3)\nv_mfma_f32_16x16x32_bf16 v[200:203], v[212:215], v[8:11], v[200:203]\nds_read_b128 v[208:211], v250 offset:2048\n" \
  "v_mfma_f32_16x16x32_bf16 v[204:207], v[212:215], v[80:83], v[204:207]\ns_waitcnt lgkmcnt(3)\nv_mfma_f32_16x16x32_bf16 v[232:235], v[216:219], v[0:3], 0\nds_read_b128 v[212:215], v250 offset:4096\nv_mfma_f32_16x16x32_bf16 v[236:239], v[216:219], v[72:75], 0\ns_waitcnt lgkmcnt(3)\n" \
  "v_mfma_f32_16x16x32_bf16 v[232:235], v[220:223], v[4:7], v[232:235]\nds_read_b128 v[216:219], v250 offset:6144\nv_mfma_f32_16x16x32_bf16 v[236:239], v[220:223], v[76:79], v[236:239]\ns_waitcnt lgkmcnt(3)\nv_mfma_f32_16x16x32_bf16 v[232:235], v[224:227], v[8:11], v[232:235]\nds_read_b128 v[220:223], v250 offset:8192\n" \
  "v_mfma_f32_16x16x32_bf16 v[236:239], v[224:227], v[80:83], v[236:239]\ns_waitcnt vmcnt(26)\ns_waitcnt lgkmcnt(3)\nv_mfma_f32_16x16x32_bf16 v[160:163], v[208:211], v[12:15], v[160:163]\nds_read_b128 v[224:227], v250 offset:10240\nv_mfma_f32_16x16x32_bf16 v[164:167], v[208:211], v[84:87], v[164:167]\n" \
  "s_waitcnt lgkmcnt(3)\nv_mfma_f32_16x16x32_bf16 v[168:171], v[212:215], v[12:15], v[168:171]\nds_read_b128 v[208:211], v250 offset:12288\nv_mfma_f32_16x16x32_bf16 v[172:175], v[212:215], v[84:87], v[172:175]\ns_waitcnt lgkmcnt(3)\nv_mfma_f32_16x16x32_bf16 v[176:179], v[216:219], v[12:15], v[176:179]\n" \
  "ds_read_b128 v[212:215], v250 offset:14336\nv_mfma_f32_16x16x32_bf16 v[180:183], v[216:219], v[84:87], v[180:183]\ns_waitcnt vmcnt(18)\ns_barrier\ns_waitcnt lgkmcnt(3)\nv_mfma_f32_16x16x32_bf16 v[184:187], v[220:223], v[12:15], v[184:187]\n" \
  "ds_read_b128 v[216:219], v250 offset:16384\nv_mfma_f32_16x16x32_bf16 v[188:191], v[220:223], v[84:87], v[188:191]\ns_add_u32 m0, s40, 65536\ns_add_u32 s49, s42, 475136\nbuffer_load_dwordx4 v251, %[rw], s49 offen sc1 lds\ns_waitcnt lgkmcnt(3)\n" \
  "v_mfma_f32_16x16x32_bf16 v[192:195], v[224:227], v[12:15], v[192:195]\nds_read_b128 v[220:223], v250 offset:18432\nv_mfma_f32_16x16x32_bf16 v[196:199], v[224:227], v[84:87], v[196:199]\ns_waitcnt vmcnt(1)\ns_mov_b64 exec, 1\nglobal_atomic_add v230, v229, %[cout] offset:384\n" \
  "s_mov_b64 exec, -1\ns_waitcnt lgkmcnt(3)\nv_mfma_f32_16x16x32_bf16 v[200:203], v[208:211], v[12:15], v[200:203]\nds_read_b128 v[224:227], v250 offset:20480\nv_mfma_f32_16x16x32_bf16 v[204:207], v[208:211], v[84:87], v[204:207]\ns_add_u32 m0, s40, 73728\n" \
  "s_add_u32 s49, s42, 483328\nbuffer_load_dwordx4 v251, %[rw], s49 offen sc1 lds\ns_waitcnt lgkmcnt(3)\nv_mfma_f32_16x16x32_bf16 v[232:235], v[212:215], v[12:15], v[232:235]\nds_read_b128 v[208:211], v250 offset:22528\nv_mfma_f32_16x16x32_bf16 v[236:239], v[212:215], v[84:87], v[236:239]\n" \
  "s_waitcnt vmcnt(26)\ns_waitcnt lgkmcnt(3)\nv_mfma_f32_16x16x32_bf16 v[160:163], v[216:219], v[16:19], v[160:163]\nds_read_b128 v[212:215], v250 offset:24576\nv_mfma_f32_16x16x32_bf16 v[164:167], v[216:219], v[88:91], v[164:167]\ns_add_u32 m0, s40, 81920\n" \
  "s_add_u32 s49, s42, 491520\nbuffer_load_dwordx4 v251, %[rw], s49 offen sc1 lds\nglobal_load_dword v228, v230, %[cin] offset:256 sc1\ns_waitcnt lgkmcnt(3)\nv_mfma_f32_16x16x32_bf16 v[168:171], v[220:223], v[16:19], v[168:171]\nds_read_b128 v[216:219], v250 offset:26624\n" \
  "v_mfma_f32_16x16x32_bf16 v[172:175], v[220:223], v[88:91], v[172:175]\ns_waitcnt lgkmcnt(3)\nv_mfma_f32_16x16x32_bf16 v[176:179], v[224:227], v[16:19], v[176:179]\nds_read_b128 v[220:223], v250 offset:28672\nv_mfma_f32_16x16x32_bf16 v[180:183], v[224:227], v[88:91], v[180:183]\ns_add_u32 m0, s40, 90112\n" \
  "s_add_u32 s49, s42, 499712\nbuffer_load_dwordx4 v251, %[rw], s49 offen sc1 lds\ns_waitcnt lgkmcnt(3)\nv_mfma_f32_16x16x32_bf16 v[184:187], v[208:211], v[16:19], v[184:187]\nds_read_b128 v[224:227], v250 offset:30720\nv_mfma_f32_16x16x32_bf16 v[188:191], v[208:211], v[88:91], v[188:191]\n" \
  "s_waitcnt lgkmcnt(3)\nv_mfma_f32_16x16x32_bf16 v[192:195], v[212:215], v[16:19], v[192:195]\nds_read_b128 v[208:211], v250 offset:32768\nv_mfma_f32_16x16x32_bf16 v[196:199], v[212:215], v[88:91], v[196:199]\ns_add_u32 m0, s40, 98304\ns_add_u32 s49, s42, 507904\n" \
  "buffer_load_dwordx4 v251, %[rw], s49 offen sc1 lds\ns_waitcnt lgkmcnt(3)\nv_mfma_f32_16x16x32_bf16 v[200:203], v[216:219], v[16:19], v[200:203]\nds_read_b128 v[212:215], v250 offset:34816\nv_mfma_f32_16x16x32_bf16 v[204:207], v[216:219], v[88:91], v[204:207]\ns_waitcnt lgkmcnt(3)\n" \
  "v_mfma_f32_16x16x32_bf16 v[232:235], v[220:223], v[16:19], v[232:235]\nds_read_b128 v[216:219], v248 offset:0\nv_mfma_f32_16x16x32_bf16 v[236:239], v[220:223], v[88:91], v[236:239]\ns_add_u32 m0, s40, 106496\ns_add_u32 s49, s42, 516096\nbuffer_load_dwordx4 v251, %[rw], s49 offen sc1 lds\n" \
  "s_waitcnt vmcnt(25)\ns_waitcnt lgkmcnt(3)\nv_mfma_f32_16x16x32_bf16 v[160:163], v[224:227], v[20:23], v[160:163]\nds_read_b128 v[220:223], v248 offset:2048\nv_mfma_f32_16x16x32_bf16 v[164:167], v[224:227], v[92:95], v[164:167]\ns_waitcnt lgkmcnt(3)\n" \
  "v_mfma_f32_16x16x32_bf16 v[168:171], v[208:211], v[20:23], v[168:171]\nds_read_b128 v[224:227], v248 offset:4096\nv_mfma_f32_16x16x32_bf16 v[172:175], v[208:211], v[92:95], v[172:175]\ns_waitcnt lgkmcnt(3)\nv_mfma_f32_16x16x32_bf16 v[176:179], v[212:215], v[20:23], v[176:179]\nds_read_b128 v[208:211], v248 offset:6144\n" \
  "v_mfma_f32_16x16x32_bf16 v[180:183], v[212:215], v[92:95], v[180:183]\ns_waitcnt lgkmcnt(3)\nv_mfma_f32_16x16x32_bf16 v[184:187], v[216:219], v[20:23], v[184:187]\nds_read_b128 v[212:215], v248 offset:8192\nv_mfma_f32_16x16x32_bf16 v[188:191], v[216:219], v[92:95], v[188:191]\ns_waitcnt lgkmcnt(3)\n" \
  "v_mfma_f32_16x16x32_bf16 v[192:195], v[220:223], v[20:23], v[192:195]\nds_read_b128 v[216:219], v248 offset:10240\nv_mfma_f32_16x16x32_bf16 v[196:199], v[220:223], v[92:95], v[196:199]\ns_waitcnt lgkmcnt(3)\nv_mfma_f32_16x16x32_bf16 v[200:203], v[224:227], v[20:23], v[200:203]\nds_read_b128 v[220:223], v248 offset:12288\n" \
  "v_mfma_f32_16x16x32_bf16 v[204:207], v[224:227], v[92:95], v[204:207]\ns_waitcnt lgkmcnt(3)\nv_mfma_f32_16x16x32_bf16 v[232:235], v[208:211], v[20:23], v[232:235]\nds_read_b128 v[224:227], v248 offset:14336\nv_mfma_f32_16x16x32_bf16 v[236:239], v[208:211], v[92:95], v[236:239]\ns_waitcnt vmcnt(21)\n" \
  "s_waitcnt lgkmcnt(3)\nv_mfma_f32_16x16x32_bf16 v[160:163], v[212:215], v[24:27], v[160:163]\nds_read_b128 v[208:211], v248 offset:16384\nv_mfma_f32_16x16x32_bf16 v[164:167], v[212:215], v[96:99], v[164:167]\ns_waitcnt lgkmcnt(3)\nv_mfma_f32_16x16x32_bf16 v[168:171], v[216:219], v[24:27], v[168:171]\n" \
  "ds_read_b128 v[212:215], v248 offset:18432\nv_mfma_f32_16x16x32_bf16 v[172:175], v[216:219], v[96:99], v[172:175]\ns_waitcnt vmcnt(3)\ns_mov_b32 s50, 0x400000\n14:\nv_readfirstlane_b32 s49, v228\n" \
  "s_sub_u32 s50, s50, 1\ns_cbranch_scc1 15f\ns_cmp_ge_u32 s49, 32\ns_cbranch_scc1 15f\ns_sleep 2\nglobal_load_dword v228, v230, %[cin] offset:256 sc1\n" \
  "s_waitcnt vmcnt(0)\ns_branch 14b\n15:\ns_barrier\ns_waitcnt lgkmcnt(3)\nv_mfma_f32_16x16x32_bf16 v[176:179], v[220:223], v[24:27], v[176:179]\n" \
  "ds_read_b128 v[216:219], v248 offset:20480\nv_mfma_f32_16x16x32_bf16 v[180:183], v[220:223], v[96:99], v[180:183]\ns_add_u32 s49, s45, 20480\nbuffer_load_dwordx4 v[40:43], v251, %[rh], s49 offen offset:0 sc1\ns_waitcnt lgkmcnt(3)\nv_mfma_f32_16x16x32_bf16 v[184:187], v[224:227], v[24:27], v[184:187]\n" \
  "ds_read_b128 v[220:223], v248 offset:22528\nv_mfma_f32_16x16x32_bf16 v[188:191], v[224:227], v[96:99], v[188:191]\ns_waitcnt lgkmcnt(3)\nv_mfma_f32_16x16x32_bf16 v[192:195], v[208:211], v[24:27], v[192:195]\nds_read_b128 v[224:227], v248 offset:24576\nv_mfma_f32_16x16x32_bf16 v[196:199], v[208:211], v[96:99], v[196:199]\n" \
  "s_add_u32 s49, s51, 20480\nbuffer_load_dwordx4 v[112:115], v251, %[rh], s49 offen offset:0 sc1\ns_waitcnt lgkmcnt(3)\nv_mfma_f32_16x16x32_bf16 v[200:203], v[212:215], v[24:27], v[200:203]\nds_read_b128 v[208:211], v248 offset:26624\nv_mfma_f32_16x16x32_bf16 v[204:207], v[212:215], v[96:99], v[204:207]\n" \
  "s_waitcnt lgkmcnt(3)\nv_mfma_f32_16x16x32_bf16 v[232:235], v[216:219], v[24:27], v[232:235]\nds_read_b128 v[212:215], v248 offset:28672\nv_mfma_f32_16x16x32_bf16 v[236:239], v[216:219], v[96:99], v[236:239]\ns_add_u32 s49, s45, 20480\nbuffer_load_dwordx4 v[44:47], v251, %[rh], s49 offen offset:2048 sc1\n" \
  "s_waitcnt vmcnt(19)\ns_waitcnt lgkmcnt(3)\nv_mfma_f32_16x16x32_bf16 v[160:163], v[220:223], v[28:31], v[160:163]\nds_read_b128 v[216:219], v248 offset:30720\nv_mfma_f32_16x16x32_bf16 v[164:167], v[220:223], v[100:103], v[164:167]\ns_waitcnt lgkmcnt(3)\n" \
  "v_mfma_f32_16x16x32_bf16 v[168:171], v[224:227], v[28:31], v[168:171]\nds_read_b128 v[220:223], v248 offset:32768\nv_mfma_f32_16x16x32_bf16 v[172:175], v[224:227], v[100:103], v[172:175]\ns_add_u32 s49, s51, 20480\nbuffer_load_dwordx4 v[116:119], v251, %[rh], s49 offen offset:2048 sc1\ns_waitcnt lgkmcnt(3)\n" \
  "v_mfma_f32_16x16x32_bf16 v[176:179], v[208:211], v[28:31], v[176:179]\nds_read_b128 v[224:227], v248 offset:34816\nv_mfma_f32_16x16x32_bf16 v[180:183], v[208:211], v[100:103], v[180:183]\ns_waitcnt lgkmcnt(3)\nv_mfma_f32_16x16x32_bf16 v[184:187], v[212:215], v[28:31], v[184:187]\nds_read_b128 v[208:211], v248 offset:36864\n" \
  "v_mfma_f32_16x16x32_bf16 v[188:191], v[212:215], v[100:103], v[188:191]\ns_add_u32 m0, s40, 114688\ns_add_u32 s49, s42, 524288\nbuffer_load_dwordx4 v251, %[rw], s49 offen sc1 lds\ns_waitcnt lgkmcnt(3)\nv_mfma_f32_16x16x32_bf16 v[192:195], v[216:219], v[28:31], v[192:195]\n" \
  "ds_read_b128 v[212:215], v248 offset:38912\nv_mfma_f32_16x16x32_bf16 v[196:199], v[216:219], v[100:103], v[196:199]\ns_waitcnt lgkmcnt(3)\nv_mfma_f32_16x16x32_bf16 v[200:203], v[220:223], v[28:31], v[200:203]\nds_read_b128 v[216:219], v248 offset:40960\nv_mfma_f32_16x16x32_bf16 v[204:207], v[220:223], v[100:103], v[204:207]\n" \
  "s_add_u32 m0, s40, 122880\ns_add_u32 s49, s42, 532480\nbuffer_load_dwordx4 v251, %[rw], s49 offen sc1 lds\ns_waitcnt lgkmcnt(3)\nv_mfma_f32_16x16x32_bf16 v[232:235], v[224:227], v[28:31], v[232:235]\nds_read_b128 v[220:223], v248 offset:43008\n" \
  "v_mfma_f32_16x16x32_bf16 v[236:239], v[224:227], v[100:103], v[236:239]\ns_waitcnt vmcnt(20)\ns_waitcnt lgkmcnt(3)\nv_mfma_f32_16x16x32_bf16 v[160:163], v[208:211], v[32:35], v[160:163]\nds_read_b128 v[224:227], v248 offset:45056\nv_mfma_f32_16x16x32_bf16 v[164:167], v[208:211], v[104:107], v[164:167]\n" \
  "s_add_u32 s49, s45, 24576\nbuffer_load_dwordx4 v[48:51], v251, %[rh], s49 offen offset:0 sc1\ns_waitcnt lgkmcnt(3)\nv_mfma_f32_16x16x32_bf16 v[168:171], v[212:215], v[32:35], v[168:171]\nds_read_b128 v[208:211], v248 offset:47104\nv_mfma_f32_16x16x32_bf16 v[172:175], v[212:215], v[104:107], v[172:175]\n" \
  "s_waitcnt lgkmcnt(3)\nv_mfma_f32_16x16x32_bf16 v[176:179], v[216:219], v[32:35], v[176:179]\nds_read_b128 v[212:215], v248 offset:49152\nv_mfma_f32_16x16x32_bf16 v[180:183], v[216:219], v[104:107], v[180:183]\ns_add_u32 s49, s51, 24576\nbuffer_load_dwordx4 v[120:123], v251, %[rh], s49 offen offset:0 sc1\n" \
  "s_waitcnt lgkmcnt(3)\nv_mfma_f32_16x16x32_bf16 v[184:187], v[220:223], v[32:35], v[184:187]\nds_read_b128 v[216:219], v248 offset:51200\nv_mfma_f32_16x16x32_bf16 v[188:191], v[220:223], v[104:107], v[188:191]\ns_waitcnt lgkmcnt(3)\nv_mfma_f32_16x16x32_bf16 v[192:195], v[224:227], v[32:35], v[192:195]\n" \
  "ds_read_b128 v[220:223], v248 offset:53248\nv_mfma_f32_16x16x32_bf16 v[196:199], v[224:227], v[104:107], v[196:199]\ns_add_u32 s49, s45, 24576\nbuffer_load_dwordx4 v[52:55], v251, %[rh], s49 offen offset:2048 sc1\ns_waitcnt lgkmcnt(3)\nv_mfma_f32_16x16x32_bf16 v[200:203], v[208:211], v[32:35], v[200:203]\n" \
  "ds_read_b128 v[224:227], v249 offset:0\nv_mfma_f32_16x16x32_bf16 v[204:207], v[208:211], v[104:107], v[204:207]\ns_waitcnt vmcnt(11)\ns_barrier\ns_waitcnt lgkmcnt(3)\nv_mfma_f32_16x16x32_bf16 v[232:235], v[212:215], v[32:35], v[232:235]\n" \
  "ds_read_b128 v[208:211], v249 offset:2048\nv_mfma_f32_16x16x32_bf16 v[236:239], v[212:215], v[104:107], v[236:239]\ns_add_u32 s49, s51, 24576\nbuffer_load_dwordx4 v[124:127], v251, %[rh], s49 offen offset:2048 sc1\ns_waitcnt vmcnt(20)\ns_waitcnt lgkmcnt(3)\n" \
  "v_mfma_f32_16x16x32_bf16 v[160:163], v[216:219], v[36:39], v[160:163]\nds_read_b128 v[212:215], v249 offset:4096\nv_mfma_f32_16x16x32_bf16 v[164:167], v[216:219], v[108:111], v[164:167]\ns_waitcnt lgkmcnt(3)\nv_mfma_f32_16x16x32_bf16 v[168:171], v[220:223], v[36:39], v[168:171]\nds_read_b128 v[216:219], v249 offset:6144\n" \
  "v_mfma_f32_16x16x32_bf16 v[172:175], v[220:223], v[108:111], v[172:175]\ns_add_u32 m0, s40, 131072\ns_add_u32 s49, s42, 540672\nbuffer_load_dwordx4 v251, %[rw], s49 offen sc1 lds\ns_waitcnt lgkmcnt(3)\nv_mfma_f32_16x16x32_bf16 v[176:179], v[224:227], v[36:39], v[176:179]\n" \
  "ds_read_b128 v[220:223], v249 offset:8192\nv_mfma_f32_16x16x32_bf16 v[180:183], v[224:227], v[108:111], v[180:183]\ns_waitcnt lgkmcnt(3)\nv_mfma_f32_16x16x32_bf16 v[184:187], v[208:211], v[36:39], v[184:187]\nds_read_b128 v[224:227], v249 offset:10240\nv_mfma_f32_16x16x32_bf16 v[188:191], v[208:211], v[108:111], v[188:191]\n" \
  "s_add_u32 m0, s40, 139264\ns_add_u32 s49, s42, 548864\nbuffer_load_dwordx4 v251, %[rw], s49 offen sc1 lds\nglobal_load_dword v228, v230, %[cin] offset:384 sc1\ns_waitcnt lgkmcnt(3)\nv_mfma_f32_16x16x32_bf16 v[192:195], v[212:215], v[36:39], v[192:195]\n" \
  "ds_read_b128 v[208:211], v249 offset:12288\nv_mfma_f32_16x16x32_bf16 v[196:199], v[212:215], v[108:111], v[196:199]\ns_waitcnt lgkmcnt(3)\nv_mfma_f32_16x16x32_bf16 v[200:203], v[216:219], v[36:39], v[200:203]\nds_read_b128 v[212:215], v249 offset:14336\nv_mfma_f32_16x16x32_bf16 v[204:207], v[216:219], v[108:111], v[204:207]\n" \
  "s_add_u32 m0, s40, 0\ns_add_u32 s49, s42, 557056\nbuffer_load_dwordx4 v251, %[rw], s49 offen sc1 lds\ns_waitcnt lgkmcnt(3)\nv_mfma_f32_16x16x32_bf16 v[232:235], v[220:223], v[36:39], v[232:235]\nds_read_b128 v[216:219], v249 offset:16384\n" \
  "v_mfma_f32_16x16x32_bf16 v[236:239], v[220:223], v[108:111], v[236:239]\ns_waitcnt vmcnt(12)\ns_waitcnt lgkmcnt(3)\nv_mfma_f32_16x16x32_bf16 v[160:163], v[224:227], v[40:43], v[160:163]\nds_read_b128 v[220:223], v249 offset:18432\nv_mfma_f32_16x16x32_bf16 v[164:167], v[224:227], v[112:115], v[164:167]\n" \
  "s_add_u32 m0, s40, 8192\ns_add_u32 s49, s42, 565248\nbuffer_load_dwordx4 v251, %[rw], s49 offen sc1 lds\ns_waitcnt lgkmcnt(3)\nv_mfma_f32_16x16x32_bf16 v[168:171], v[208:211], v[40:43], v[168:171]\nds_read_b128 v[224:227], v249 offset:20480\n" \
  "v_mfma_f32_16x16x32_bf16 v[172:175], v[208:211], v[112:115], v[172:175]\ns_waitcnt lgkmcnt(3)\nv_mfma_f32_16x16x32_bf16 v[176:179], v[212:215], v[40:43], v[176:179]\nds_read_b128 v[208:211], v249 offset:22528\nv_mfma_f32_16x16x32_bf16 v[180:183], v[212:215], v[112:115], v[180:183]\ns_add_u32 m0, s40, 16384\n" \
  "s_add_u32 s49, s42, 573440\nbuffer_load_dwordx4 v251, %[rw], s49 offen sc1 lds\ns_waitcnt lgkmcnt(3)\nv_mfma_f32_16x16x32_bf16 v[184:187], v[216:219], v[40:43], v[184:187]\nds_read_b128 v[212:215], v249 offset:24576\nv_mfma_f32_16x16x32_bf16 v[188:191], v[216:219], v[112:115], v[188:191]\n" \
  "s_waitcnt lgkmcnt(3)\nv_mfma_f32_16x16x32_bf16 v[192:195], v[220:223], v[40:43], v[192:195]\nds_read_b128 v[216:219], v249 offset:26624\nv_mfma_f32_16x16x32_bf16 v[196:199], v[220:223], v[112:115], v[196:199]\ns_add_u32 m0, s40, 24576\ns_add_u32 s49, s42, 581632\n" \
  "buffer_load_dwordx4 v251, %[rw], s49 offen sc1 lds\ns_waitcnt lgkmcnt(3)\nv_mfma_f32_16x16x32_bf16 v[200:203], v[224:227], v[40:43], v[200:203]\nds_read_b128 v[220:223], v249 offset:28672\nv_mfma_f32_16x16x32_bf16 v[204:207], v[224:227], v[112:115], v[204:207]\ns_waitcnt lgkmcnt(3)\n" \
  "v_mfma_f32_16x16x32_bf16 v[232:235], v[208:211], v[40:43], v[232:235]\nds_read_b128 v[224:227], v249 offset:30720\nv_mfma_f32_16x16x32_bf16 v[236:239], v[208:211], v[112:115], v[236:239]\ns_add_u32 m0, s40, 32768\ns_add_u32 s49, s42, 589824\nbuffer_load_dwordx4 v251, %[rw], s49 offen sc1 lds\n" \
  "s_waitcnt vmcnt(14)\ns_waitcnt lgkmcnt(3)\nv_mfma_f32_16x16x32_bf16 v[160:163], v[212:215], v[44:47], v[160:163]\nds_read_b128 v[208:211], v249 offset:32768\nv_mfma_f32_16x16x32_bf16 v[164:167], v[212:215], v[116:119], v[164:167]\ns_waitcnt vmcnt(5)\n" \
  "s_mov_b32 s50, 0x400000\n16:\nv_readfirstlane_b32 s49, v228\ns_sub_u32 s50, s50, 1\ns_cbranch_scc1 17f\ns_cmp_ge_u32 s49, 32\n" \
  "s_cbranch_scc1 17f\ns_sleep 2\nglobal_load_dword v228, v230, %[cin] offset:384 sc1\ns_waitcnt vmcnt(0)\ns_branch 16b\n17:\n" \
  "s_barrier\ns_waitcnt lgkmcnt(3)\nv_mfma_f32_16x16x32_bf16 v[168:171], v[216:219], v[44:47], v[168:171]\nds_read_b128 v[212:215], v249 offset:34816\nv_mfma_f32_16x16x32_bf16 v[172:175], v[216:219], v[116:119], v[172:175]\ns_add_u32 s49, s45, 28672\n" \
  "buffer_load_dwordx4 v[56:59], v251, %[rh], s49 offen offset:0 sc1\ns_waitcnt lgkmcnt(3)\nv_mfma_f32_16x16x32_bf16 v[176:179], v[220:223], v[44:47], v[176:179]\nds_read_b128 v[216:219], v249 offset:36864\nv_mfma_f32_16x16x32_bf16 v[180:183], v[220:223], v[116:119], v[180:183]\ns_waitcnt lgkmcnt(3)\n" \
  "v_mfma_f32_16x16x32_bf16 v[184:187], v[224:227], v[44:47], v[184:187]\nds_read_b128 v[220:223], v249 offset:38912\nv_mfma_f32_16x16x32_bf16 v[188:191], v[224:227], v[116:119], v[188:191]\ns_add_u32 s49, s51, 28672\nbuffer_load_dwordx4 v[128:131], v251, %[rh], s49 offen offset:0 sc1\ns_waitcnt lgkmcnt(3)\n" \
  "v_mfma_f32_16x16x32_bf16 v[192:195], v[208:211], v[44:47], v[192:195]\nds_read_b128 v[224:227], v249 offset:40960\nv_mfma_f32_16x16x32_bf16 v[196:199], v[208:211], v[116:119], v[196:199]\ns_waitcnt lgkmcnt(3)\nv_mfma_f32_16x16x32_bf16 v[200:203], v[212:215], v[44:47], v[200:203]\nds_read_b128 v[208:211], v249 offset:43008\n" \
  "v_mfma_f32_16x16x32_bf16 v[204:207], v[212:215], v[116:119], v[204:207]\ns_add_u32 s49, s45, 28672\nbuffer_load_dwordx4 v[60:63], v251, %[rh], s49 offen offset:2048 sc1\ns_waitcnt lgkmcnt(3)\nv_mfma_f32_16x16x32_bf16 v[232:235], v[216:219], v[44:47], v[232:235]\nds_read_b128 v[212:215], v249 offset:45056\n" \
  "v_mfma_f32_16x16x32_bf16 v[236:239], v[216:219], v[116:119], v[236:239]\ns_waitcnt vmcnt(13)\ns_waitcnt lgkmcnt(3)\nv_mfma_f32_16x16x32_bf16 v[160:163], v[220:223], v[48:51], v[160:163]\nds_read_b128 v[216:219], v249 offset:47104\nv_mfma_f32_16x16x32_bf16 v[164:167], v[220:223], v[120:123], v[164:167]\n" \
  "s_add_u32 s49, s51, 28672\nbuffer_load_dwordx4 v[132:135], v251, %[rh], s49 offen offset:2048 sc1\ns_waitcnt lgkmcnt(3)\nv_mfma_f32_16x16x32_bf16 v[168:171], v[224:227], v[48:51], v[168:171]\nds_read_b128 v[220:223], v249 offset:49152\nv_mfma_f32_16x16x32_bf16 v[172:175], v[224:227], v[120:123], v[172:175]\n" \
  "s_waitcnt lgkmcnt(3)\nv_mfma_f32_16x16x32_bf16 v[176:179], v[208:211], v[48:51], v[176:179]\nds_read_b128 v[224:227], v249 offset:51200\nv_mfma_f32_16x16x32_bf16 v[180:183], v[208:211], v[120:123], v[180:183]\ns_add_u32 s49, s45, 32768\nbuffer_load_dwordx4 v[64:67], v251, %[rh], s49 offen offset:0 sc1\n" \
  "s_waitcnt lgkmcnt(3)\nv_mfma_f32_16x16x32_bf16 v[184:187], v[212:215], v[48:51], v[184:187]\nds_read_b128 v[208:211], v249 offset:53248\nv_mfma_f32_16x16x32_bf16 v[188:191], v[212:215], v[120:123], v[188:191]\ns_waitcnt lgkmcnt(3)\nv_mfma_f32_16x16x32_bf16 v[192:195], v[216:219], v[48:51], v[192:195]\n" \
  "ds_read_b128 v[212:215], v250 offset:0\nv_mfma_f32_16x16x32_bf16 v[196:199], v[216:219], v[120:123], v[196:199]\ns_add_u32 s49, s51, 32768\nbuffer_load_dwordx4 v[136:139], v251, %[rh], s49 offen offset:0 sc1\ns_waitcnt lgkmcnt(3)\nv_mfma_f32_16x16x32_bf16 v[200:203], v[220:223], v[48:51], v[200:203]\n" \
  "ds_read_b128 v[216:219], v250 offset:2048\nv_mfma_f32_16x16x32_bf16 v[204:207], v[220:223], v[120:123], v[204:207]\ns_waitcnt lgkmcnt(3)\nv_mfma_f32_16x16x32_bf16 v[232:235], v[224:227], v[48:51], v[232:235]\nds_read_b128 v[220:223], v250 offset:4096\nv_mfma_f32_16x16x32_bf16 v[236:239], v[224:227], v[120:123], v[236:239]\n" \
  "s_add_u32 s49, s45, 32768\nbuffer_load_dwordx4 v[68:71], v251, %[rh], s49 offen offset:2048 sc1\ns_waitcnt vmcnt(15)\ns_waitcnt lgkmcnt(3)\nv_mfma_f32_16x16x32_bf16 v[160:163], v[208:211], v[52:55], v[160:163]\nds_read_b128 v[224:227], v250 offset:6144\n" \
  "v_mfma_f32_16x16x32_bf16 v[164:167], v[208:211], v[124:127], v[164:167]\ns_waitcnt lgkmcnt(3)\nv_mfma_f32_16x16x32_bf16 v[168:171], v[212:215], v[52:55], v[168:171]\nds_read_b128 v[208:211], v250 offset:8192\nv_mfma_f32_16x16x32_bf16 v[172:175], v[212:215], v[124:127], v[172:175]\ns_add_u32 s49, s51, 32768\n" \
  "buffer_load_dwordx4 v[140:143], v251, %[rh], s49 offen offset:2048 sc1\ns_waitcnt lgkmcnt(3)\nv_mfma_f32_16x16x32_bf16 v[176:179], v[216:219], v[52:55], v[176:179]\nds_read_b128 v[212:215], v250 offset:10240\nv_mfma_f32_16x16x32_bf16 v[180:183], v[216:219], v[124:127], v[180:183]\ns_waitcnt vmcnt(11)\n" \
  "s_barrier\ns_waitcnt lgkmcnt(3)\nv_mfma_f32_16x16x32_bf16 v[184:187], v[220:223], v[52:55], v[184:187]\nds_read_b128 v[216:219], v250 offset:12288\nv_mfma_f32_16x16x32_bf16 v[188:191], v[220:223], v[124:127], v[188:191]\ns_add_u32 m0, s40, 40960\n" \
  "s_add_u32 s49, s42, 598016\nbuffer_load_dwordx4 v251, %[rw], s49 offen sc1 lds\ns_waitcnt lgkmcnt(3)\nv_mfma_f32_16x16x32_bf16 v[192:195], v[224:227], v[52:55], v[192:195]\nds_read_b128 v[220:223], v250 offset:14336\nv_mfma_f32_16x16x32_bf16 v[196:199], v[224:227], v[124:127], v[196:199]\n" \
  "s_waitcnt lgkmcnt(3)\nv_mfma_f32_16x16x32_bf16 v[200:203], v[208:211], v[52:55], v[200:203]\nds_read_b128 v[224:227], v250 offset:16384\nv_mfma_f32_16x16x32_bf16 v[204:207], v[208:211], v[124:127], v[204:207]\ns_add_u32 m0, s40, 49152\ns_add_u32 s49, s42, 606208\n" \
  "buffer_load_dwordx4 v251, %[rw], s49 offen sc1 lds\ns_waitcnt lgkmcnt(3)\nv_mfma_f32_16x16x32_bf16 v[232:235], v[212:215], v[52:55], v[232:235]\nds_read_b128 v[208:211], v250 offset:18432\nv_mfma_f32_16x16x32_bf16 v[236:239], v[212:215], v[124:127], v[236:239]\ns_waitcnt vmcnt(8)\n" \
  "s_waitcnt lgkmcnt(3)\nv_mfma_f32_16x16x32_bf16 v[232:235], v[216:219], v[56:59], v[232:235]\nds_read_b128 v[212:215], v250 offset:20480\nv_mfma_f32_16x16x32_bf16 v[236:239], v[216:219], v[128:131], v[236:239]\ns_add_u32 m0, s40, 57344\ns_add_u32 s49, s42, 614400\n" \
  "buffer_load_dwordx4 v251, %[rw], s49 offen sc1 lds\ns_waitcnt vmcnt(7)\ns_waitcnt lgkmcnt(3)\nv_mfma_f32_16x16x32_bf16 v[232:235], v[220:223], v[60:63], v[232:235]\nds_read_b128 v[216:219], v250 offset:22528\nv_mfma_f32_16x16x32_bf16 v[236:239], v[220:223], v[132:135], v[236:239]\n" \
  "s_waitcnt vmcnt(5)\ns_waitcnt lgkmcnt(3)\nv_mfma_f32_16x16x32_bf16 v[232:235], v[224:227], v[64:67], v[232:235]\nds_read_b128 v[220:223], v250 offset:24576\nv_mfma_f32_16x16x32_bf16 v[236:239], v[224:227], v[136:139], v[236:239]\ns_add_u32 m0, s40, 65536\n" \
  "s_add_u32 s49, s42, 622592\nbuffer_load_dwordx4 v251, %[rw], s49 offen sc1 lds\ns_waitcnt vmcnt(4)\ns_waitcnt lgkmcnt(3)\nv_mfma_f32_16x16x32_bf16 v[232:235], v[208:211], v[68:71], v[232:235]\nds_read_b128 v[224:227], v250 offset:26624\n" \
  "v_mfma_f32_16x16x32_bf16 v[236:239], v[208:211], v[140:143], v[236:239]\ns_waitcnt lgkmcnt(3)\nv_mfma_f32_16x16x32_bf16 v[160:163], v[212:215], v[56:59], v[160:163]\nds_read_b128 v[208:211], v250 offset:28672\nv_mfma_f32_16x16x32_bf16 v[164:167], v[212:215], v[128:131], v[164:167]\ns_add_u32 m0, s40, 73728\n" \
  "s_add_u32 s49, s42, 630784\nbuffer_load_dwordx4 v251, %[rw], s49 offen sc1 lds\ns_waitcnt lgkmcnt(3)\nv_mfma_f32_16x16x32_bf16 v[168:171], v[216:219], v[56:59], v[168:171]\nds_read_b128 v[212:215], v250 offset:30720\nv_mfma_f32_16x16x32_bf16 v[172:175], v[216:219], v[128:131], v[172:175]\n" \
  "s_waitcnt lgkmcnt(3)\nv_mfma_f32_16x16x32_bf16 v[160:163], v[220:223], v[60:63], v[160:163]\nds_read_b128 v[216:219], v250 offset:32768\nv_mfma_f32_16x16x32_bf16 v[164:167], v[220:223], v[132:135], v[164:167]\ns_add_u32 m0, s40, 81920\ns_add_u32 s49, s42, 638976\n" \
  "buffer_load_dwordx4 v251, %[rw], s49 offen sc1 lds\ns_waitcnt lgkmcnt(3)\nv_mfma_f32_16x16x32_bf16 v[168:171], v[224:227], v[60:63], v[168:171]\nds_read_b128 v[220:223], v250 offset:34816\nds_write_b128 v253, v[236:239] offset:49152\nv_mfma_f32_16x16x32_bf16 v[172:175], v[224:227], v[132:135], v[172:175]\n" \
  "s_waitcnt lgkmcnt(4)\nv_mfma_f32_16x16x32_bf16 v[160:163], v[208:211], v[64:67], v[160:163]\nds_read_b128 v[224:227], v248 offset:0\nv_mfma_f32_16x16x32_bf16 v[164:167], v[208:211], v[136:139], v[164:167]\ns_add_u32 m0, s40, 90112\ns_add_u32 s49, s42, 647168\n" \
  "buffer_load_dwordx4 v251, %[rw], s49 offen sc1 lds\ns_waitcnt lgkmcnt(4)\nv_mfma_f32_16x16x32_bf16 v[168:171], v[212:215], v[64:67], v[168:171]\nds_read_b128 v[208:211], v248 offset:2048\nv_mfma_f32_16x16x32_bf16 v[172:175], v[212:215], v[136:139], v[172:175]\ns_waitcnt lgkmcnt(4)\n" \
  "v_mfma_f32_16x16x32_bf16 v[160:163], v[216:219], v[68:71], v[160:163]\nds_read_b128 v[212:215], v248 offset:4096\nv_mfma_f32_16x16x32_bf16 v[164:167], v[216:219], v[140:143], v[164:167]\ns_add_u32 m0, s40, 98304\ns_add_u32 s49, s42, 655360\nbuffer_load_dwordx4 v251, %[rw], s49 offen sc1 lds\n" \
  "s_waitcnt lgkmcnt(4)\nv_mfma_f32_16x16x32_bf16 v[168:171], v[220:223], v[68:71], v[168:171]\nds_read_b128 v[216:219], v248 offset:6144\nv_mfma_f32_16x16x32_bf16 v[172:175], v[220:223], v[140:143], v[172:175]\ns_waitcnt vmcnt(18)\ns_waitcnt lgkmcnt(4)\n" \
  "s_barrier\ns_waitcnt lgkmcnt(3)\nv_mfma_f32_16x16x32_bf16 v[176:179], v[224:227], v[56:59], v[176:179]\nds_read_b128 v[220:223], v248 offset:8192\nds_read_b128 v[236:239], v254 offset:49152\nv_mfma_f32_16x16x32_bf16 v[180:183], v[224:227], v[128:131], v[180:183]\n" \
  "s_waitcnt lgkmcnt(4)\nv_mfma_f32_16x16x32_bf16 v[184:187], v[208:211], v[56:59], v[184:187]\nds_read_b128 v[224:227], v248 offset:10240\nv_mfma_f32_16x16x32_bf16 v[188:191], v[208:211], v[128:131], v[188:191]\ns_waitcnt lgkmcnt(4)\nv_mfma_f32_16x16x32_bf16 v[176:179], v[212:215], v[60:63], v[176:179]\n" \
  "ds_read_b128 v[208:211], v248 offset:12288\ns_waitcnt lgkmcnt(2)\nv_add_f32 v232, v232, v236\nv_mfma_f32_16x16x32_bf16 v[180:183], v[212:215], v[132:135], v[180:183]\nv_add_f32 v233, v233, v237\nv_add_f32 v234, v234, v238\n" \
  "s_waitcnt lgkmcnt(4)\nv_mfma_f32_16x16x32_bf16 v[184:187], v[216:219], v[60:63], v[184:187]\nds_read_b128 v[212:215], v248 offset:14336\nds_write_b128 v253, v[164:167] offset:0\nv_mfma_f32_16x16x32_bf16 v[188:191], v[216:219], v[132:135], v[188:191]\nds_write_b128 v253, v[172:175] offset:8192\n" \
  "v_add_f32 v235, v235, v239\ns_waitcnt lgkmcnt(6)\nv_mfma_f32_16x16x32_bf16 v[176:179], v[220:223], v[64:67], v[176:179]\nds_read_b128 v[216:219], v248 offset:16384\nv_mov_b32 v236, v232\nv_mfma_f32_16x16x32_bf16 v[180:183], v[220:223], v[136:139], v[180:183]\n" \
  "v_mov_b32 v237, v233\nv_mov_b32 v238, v234\ns_waitcnt lgkmcnt(5)\nv_mfma_f32_16x16x32_bf16 v[184:187], v[224:227], v[64:67], v[184:187]\nds_read_b128 v[220:223], v248 offset:18432\nv_mov_b32 v239, v235\n" \
  "v_mfma_f32_16x16x32_bf16 v[188:191], v[224:227], v[136:139], v[188:191]\ns_nop 1\nv_permlane32_swap_b32 v232, v236\ns_waitcnt vmcnt(16)\ns_waitcnt lgkmcnt(2)\ns_barrier\n" \
  "s_waitcnt lgkmcnt(5)\nv_mfma_f32_16x16x32_bf16 v[176:179], v[208:211], v[68:71], v[176:179]\nds_read_b128 v[224:227], v248 offset:20480\nds_read_b128 v[164:167], v254 offset:0\nv_mfma_f32_16x16x32_bf16 v[180:183], v[208:211], v[140:143], v[180:183]\nds_read_b128 v[172:175], v254 offset:8192\n" \
  "v_permlane32_swap_b32 v233, v237\ns_waitcnt lgkmcnt(7)\nv_mfma_f32_16x16x32_bf16 v[184:187], v[212:215], v[68:71], v[184:187]\nds_read_b128 v[208:211], v248 offset:22528\nv_permlane32_swap_b32 v234, v238\nv_mfma_f32_16x16x32_bf16 v[188:191], v[212:215], v[140:143], v[188:191]\n" \
  "v_permlane32_swap_b32 v235, v239\nv_cvt_pk_bf16_f32  v240, v232, v236\ns_waitcnt lgkmcnt(5)\nv_mfma_f32_16x16x32_bf16 v[192:195], v[216:219], v[56:59], v[192:195]\nds_read_b128 v[212:215], v248 offset:24576\nv_cvt_pk_bf16_f32  v241, v233, v237\n" \
  "v_mfma_f32_16x16x32_bf16 v[196:199], v[216:219], v[128:131], v[196:199]\nv_cvt_pk_bf16_f32  v242, v234, v238\nv_cvt_pk_bf16_f32  v243, v235, v239\ns_waitcnt lgkmcnt(5)\nv_mfma_f32_16x16x32_bf16 v[200:203], v[220:223], v[56:59], v[200:203]\nds_read_b128 v[216:219], v248 offset:26624\n" \
  "s_mov_b32 exec_hi, 0\nbuffer_store_dwordx4 v[240:243], v251, %[rp], s55 offen sc1\ns_mov_b32 exec_hi, -1\nv_mfma_f32_16x16x32_bf16 v[204:207], v[220:223], v[128:131], v[204:207]\ns_waitcnt lgkmcnt(5)\nv_mfma_f32_16x16x32_bf16 v[192:195], v[224:227], v[60:63], v[192:195]\n" \
  "ds_read_b128 v[220:223], v248 offset:28672\nv_mfma_f32_16x16x32_bf16 v[196:199], v[224:227], v[132:135], v[196:199]\ns_waitcnt lgkmcnt(3)\nv_mfma_f32_16x16x32_bf16 v[200:203], v[208:211], v[60:63], v[200:203]\nds_read_b128 v[224:227], v248 offset:30720\nds_write_b128 v253, v[180:183] offset:16384\n" \
  "v_mfma_f32_16x16x32_bf16 v[204:207], v[208:211], v[132:135], v[204:207]\nds_write_b128 v253, v[188:191] offset:24576\ns_waitcnt lgkmcnt(7)\nv_add_f32 v160, v160, v164\ns_waitcnt lgkmcnt(5)\nv_mfma_f32_16x16x32_bf16 v[192:195], v[212:215], v[64:67], v[192:195]\n" \
  "ds_read_b128 v[208:211], v248 offset:32768\nv_add_f32 v161, v161, v165\nv_mfma_f32_16x16x32_bf16 v[196:199], v[212:215], v[136:139], v[196:199]\nv_add_f32 v162, v162, v166\nv_add_f32 v163, v163, v167\ns_waitcnt lgkmcnt(5)\n" \
  "v_mfma_f32_16x16x32_bf16 v[200:203], v[216:219], v[64:67], v[200:203]\nds_read_b128 v[212:215], v248 offset:34816\nv_add_f32 v168, v168, v172\nv_mfma_f32_16x16x32_bf16 v[204:207], v[216:219], v[136:139], v[204:207]\nv_add_f32 v169, v169, v173\nv_add_f32 v170, v170, v174\n" \
  "s_waitcnt vmcnt(7)\ns_waitcnt lgkmcnt(2)\ns_barrier\ns_waitcnt lgkmcnt(5)\nv_mfma_f32_16x16x32_bf16 v[192:195], v[220:223], v[68:71], v[192:195]\nds_read_b128 v[216:219], v248 offset:36864\n" \
  "ds_read_b128 v[180:183], v254 offset:16384\nv_mfma_f32_16x16x32_bf16 v[196:199], v[220:223], v[140:143], v[196:199]\nds_read_b128 v[188:191], v254 offset:24576\nv_add_f32 v171, v171, v175\ns_waitcnt lgkmcnt(7)\nv_mfma_f32_16x16x32_bf16 v[200:203], v[224:227], v[68:71], v[200:203]\n" \
  "ds_read_b128 v[220:223], v248 offset:38912\nv_cvt_pk_bf16_f32 v160, v160, v168\nv_mfma_f32_16x16x32_bf16 v[204:207], v[224:227], v[140:143], v[204:207]\nv_cvt_pk_bf16_f32 v161, v161, v169\nv_cvt_pk_bf16_f32 v162, v162, v170\ns_waitcnt lgkmcnt(5)\n" \
  "v_mfma_f32_16x16x32_bf16 v[144:147], v[208:211], v[0:3], 0\nds_read_b128 v[224:227], v248 offset:40960\nv_cvt_pk_bf16_f32 v163, v163, v171\nv_mfma_f32_16x16x32_bf16 v[148:151], v[208:211], v[72:75], 0\nbuffer_store_dwordx4 v[160:163], v251, %[rp], s52 offen sc1\ns_waitcnt lgkmcnt(5)\n" \
  "v_mfma_f32_16x16x32_bf16 v[152:155], v[212:215], v[0:3], 0\nds_read_b128 v[208:211], v248 offset:43008\nv_mfma_f32_16x16x32_bf16 v[156:159], v[212:215], v[72:75], 0\ns_waitcnt lgkmcnt(5)\nv_mfma_f32_16x16x32_bf16 v[144:147], v[216:219], v[4:7], v[144:147]\nds_read_b128 v[212:215], v248 offset:45056\n" \
  "v_mfma_f32_16x16x32_bf16 v[148:151], v[216:219], v[76:79], v[148:151]\ns_waitcnt lgkmcnt(4)\nv_add_f32 v176, v176, v180\nv_add_f32 v177, v177, v181\ns_waitcnt lgkmcnt(3)\nv_mfma_f32_16x16x32_bf16 v[152:155], v[220:223], v[4:7], v[152:155]\n" \
  "ds_read_b128 v[216:219], v248 offset:47104\nds_write_b128 v253, v[196:199] offset:32768\nv_mfma_f32_16x16x32_bf16 v[156:159], v[220:223], v[76:79], v[156:159]\nds_write_b128 v253, v[204:207] offset:40960\nv_add_f32 v178, v178, v182\ns_waitcnt lgkmcnt(5)\n" \
  "v_mfma_f32_16x16x32_bf16 v[144:147], v[224:227], v[8:11], v[144:147]\nds_read_b128 v[220:223], v248 offset:49152\nv_add_f32 v179, v179, v183\nv_mfma_f32_16x16x32_bf16 v[148:151], v[224:227], v[80:83], v[148:151]\nv_add_f32 v184, v184, v188\nv_add_f32 v185, v185, v189\n" \
  "s_waitcnt vmcnt(2)\ns_waitcnt lgkmcnt(1)\ns_barrier\ns_waitcnt lgkmcnt(5)\nv_mfma_f32_16x16x32_bf16 v[152:155], v[208:211], v[8:11], v[152:155]\nds_read_b128 v[224:227], v248 offset:51200\n" \
  "ds_read_b128 v[196:199], v254 offset:32768\nv_mfma_f32_16x16x32_bf16 v[156:159], v[208:211], v[80:83], v[156:159]\nds_read_b128 v[204:207], v254 offset:40960\nv_add_f32 v186, v186, v190\ns_waitcnt lgkmcnt(7)\nv_mfma_f32_16x16x32_bf16 v[144:147], v[212:215], v[12:15], v[144:147]\n" \
  "ds_read_b128 v[208:211], v248 offset:53248\nv_add_f32 v187, v187, v191\nv_mfma_f32_16x16x32_bf16 v[148:151], v[212:215], v[84:87], v[148:151]\nv_cvt_pk_bf16_f32 v176, v176, v184\nv_cvt_pk_bf16_f32 v177, v177, v185\ns_waitcnt lgkmcnt(7)\n" \
  "v_mfma_f32_16x16x32_bf16 v[152:155], v[216:219], v[12:15], v[152:155]\nds_read_b128 v[212:215], v249 offset:0\nv_cvt_pk_bf16_f32 v178, v178, v186\nv_mfma_f32_16x16x32_bf16 v[156:159], v[216:219], v[84:87], v[156:159]\nv_cvt_pk_bf16_f32 v179, v179, v187\nbuffer_store_dwordx4 v[176:179], v251, %[rp], s53 offen sc1\n" \
  "s_waitcnt lgkmcnt(5)\nv_mfma_f32_16x16x32_bf16 v[144:147], v[220:223], v[16:19], v[144:147]\nds_read_b128 v[216:219], v249 offset:2048\nv_mfma_f32_16x16x32_bf16 v[148:151], v[220:223], v[88:91], v[148:151]\ns_waitcnt lgkmcnt(5)\nv_mfma_f32_16x16x32_bf16 v[152:155], v[224:227], v[16:19], v[152:155]\n" \
  "ds_read_b128 v[220:223], v249 offset:4096\nv_mfma_f32_16x16x32_bf16 v[156:159], v[224:227], v[88:91], v[156:159]\ns_waitcnt lgkmcnt(4)\nv_add_f32 v192, v192, v196\ns_waitcnt lgkmcnt(3)\nv_mfma_f32_16x16x32_bf16 v[144:147], v[208:211], v[20:23], v[144:147]\n" \
  "ds_read_b128 v[224:227], v249 offset:6144\nv_add_f32 v193, v193, v197\nv_mfma_f32_16x16x32_bf16 v[148:151], v[208:211], v[92:95], v[148:151]\nv_add_f32 v194, v194, v198\nv_add_f32 v195, v195, v199\ns_waitcnt lgkmcnt(3)\n" \
  "v_mfma_f32_16x16x32_bf16 v[152:155], v[212:215], v[20:23], v[152:155]\nds_read_b128 v[208:211], v249 offset:8192\nv_add_f32 v200, v200, v204\nv_mfma_f32_16x16x32_bf16 v[156:159], v[212:215], v[92:95], v[156:159]\nv_add_f32 v201, v201, v205\nv_add_f32 v202, v202, v206\n" \
  "s_waitcnt lgkmcnt(3)\nv_mfma_f32_16x16x32_bf16 v[144:147], v[216:219], v[24:27], v[144:147]\nds_read_b128 v[212:215], v249 offset:10240\nv_add_f32 v203, v203, v207\nv_mfma_f32_16x16x32_bf16 v[148:151], v[216:219], v[96:99], v[148:151]\nv_cvt_pk_bf16_f32 v192, v192, v200\n" \
  "v_cvt_pk_bf16_f32 v193, v193, v201\ns_waitcnt lgkmcnt(3)\nv_mfma_f32_16x16x32_bf16 v[152:155], v[220:223], v[24:27], v[152:155]\nds_read_b128 v[216:219], v249 offset:12288\nv_cvt_pk_bf16_f32 v194, v194, v202\nv_mfma_f32_16x16x32_bf16 v[156:159], v[220:223], v[96:99], v[156:159]\n" \
  "v_cvt_pk_bf16_f32 v195, v195, v203\nbuffer_store_dwordx4 v[192:195], v251, %[rp], s54 offen sc1\ns_waitcnt lgkmcnt(3)\nv_mfma_f32_16x16x32_bf16 v[144:147], v[224:227], v[28:31], v[144:147]\nds_read_b128 v[220:223], v249 offset:14336\nv_mfma_f32_16x16x32_bf16 v[148:151], v[224:227], v[100:103], v[148:151]\n" \
  "s_waitcnt lgkmcnt(3)\nv_mfma_f32_16x16x32_bf16 v[152:155], v[208:211], v[28:31], v[152:155]\nds_read_b128 v[224:227], v249 offset:16384\nv_mfma_f32_16x16x32_bf16 v[156:159], v[208:211], v[100:103], v[156:159]\ns_waitcnt lgkmcnt(3)\nv_mfma_f32_16x16x32_bf16 v[144:147], v[212:215], v[32:35], v[144:147]\n" \
  "ds_read_b128 v[208:211], v249 offset:18432\nv_mfma_f32_16x16x32_bf16 v[148:151], v[212:215], v[104:107], v[148:151]\ns_waitcnt lgkmcnt(3)\nv_mfma_f32_16x16x32_bf16 v[152:155], v[216:219], v[32:35], v[152:155]\nds_read_b128 v[212:215], v249 offset:20480\nv_mfma_f32_16x16x32_bf16 v[156:159], v[216:219], v[104:107], v[156:159]\n" \
  "s_waitcnt lgkmcnt(3)\nv_mfma_f32_16x16x32_bf16 v[144:147], v[220:223], v[36:39], v[144:147]\nds_read_b128 v[216:219], v249 offset:22528\nv_mfma_f32_16x16x32_bf16 v[148:151], v[220:223], v[108:111], v[148:151]\ns_waitcnt lgkmcnt(3)\nv_mfma_f32_16x16x32_bf16 v[152:155], v[224:227], v[36:39], v[152:155]\n" \
  "ds_read_b128 v[220:223], v249 offset:24576\nv_mfma_f32_16x16x32_bf16 v[156:159], v[224:227], v[108:111], v[156:159]\ns_waitcnt lgkmcnt(3)\nv_mfma_f32_16x16x32_bf16 v[144:147], v[208:211], v[40:43], v[144:147]\nds_read_b128 v[224:227], v249 offset:26624\nv_mfma_f32_16x16x32_bf16 v[148:151], v[208:211], v[112:115], v[148:151]\n" \
  "s_waitcnt lgkmcnt(3)\nv_mfma_f32_16x16x32_bf16 v[152:155], v[212:215], v[40:43], v[152:155]\nds_read_b128 v[208:211], v249 offset:28672\nv_mfma_f32_16x16x32_bf16 v[156:159], v[212:215], v[112:115], v[156:159]\ns_waitcnt lgkmcnt(3)\nv_mfma_f32_16x16x32_bf16 v[144:147], v[216:219], v[44:47], v[144:147]\n" \
  "ds_read_b128 v[212:215], v249 offset:30720\nv_mfma_f32_16x16x32_bf16 v[148:151], v[216:219], v[116:119], v[148:151]\ns_waitcnt lgkmcnt(3)\nv_mfma_f32_16x16x32_bf16 v[152:155], v[220:223], v[44:47], v[152:155]\nds_read_b128 v[216:219], v249 offset:32768\nv_mfma_f32_16x16x32_bf16 v[156:159], v[220:223], v[116:119], v[156:159]\n" \
  "s_waitcnt vmcnt(0)\ns_mov_b64 exec, 1\nglobal_atomic_add v230, v229, %[cfin]\ns_mov_b64 exec, -1\ns_waitcnt lgkmcnt(3)\nv_mfma_f32_16x16x32_bf16 v[144:147], v[224:227], v[48:51], v[144:147]\n" \
  "ds_read_b128 v[220:223], v249 offset:34816\nv_mfma_f32_16x16x32_bf16 v[148:151], v[224:227], v[120:123], v[148:151]\ns_waitcnt lgkmcnt(3)\nv_mfma_f32_16x16x32_bf16 v[152:155], v[208:211], v[48:51], v[152:155]\nds_read_b128 v[224:227], v249 offset:36864\nv_mfma_f32_16x16x32_bf16 v[156:159], v[208:211], v[120:123], v[156:159]\n" \
  "s_waitcnt lgkmcnt(3)\nv_mfma_f32_16x16x32_bf16 v[144:147], v[212:215], v[52:55], v[144:147]\nds_read_b128 v[208:211], v249 offset:38912\nv_mfma_f32_16x16x32_bf16 v[148:151], v[212:215], v[124:127], v[148:151]\ns_waitcnt lgkmcnt(3)\nv_mfma_f32_16x16x32_bf16 v[152:155], v[216:219], v[52:55], v[152:155]\n" \
  "ds_read_b128 v[212:215], v249 offset:40960\nv_mfma_f32_16x16x32_bf16 v[156:159], v[216:219], v[124:127], v[156:159]\ns_waitcnt lgkmcnt(3)\nv_mfma_f32_16x16x32_bf16 v[144:147], v[220:223], v[56:59], v[144:147]\nds_read_b128 v[216:219], v249 offset:43008\nv_mfma_f32_16x16x32_bf16 v[148:151], v[220:223], v[128:131], v[148:151]\n" \
  "s_waitcnt lgkmcnt(3)\nv_mfma_f32_16x16x32_bf16 v[152:155], v[224:227], v[56:59], v[152:155]\nds_read_b128 v[220:223], v249 offset:45056\nv_mfma_f32_16x16x32_bf16 v[156:159], v[224:227], v[128:131], v[156:159]\ns_waitcnt lgkmcnt(3)\nv_mfma_f32_16x16x32_bf16 v[144:147], v[208:211], v[60:63], v[144:147]\n" \
  "ds_read_b128 v[224:227], v249 offset:47104\nv_mfma_f32_16x16x32_bf16 v[148:151], v[208:211], v[132:135], v[148:151]\ns_waitcnt lgkmcnt(3)\nv_mfma_f32_16x16x32_bf16 v[152:155], v[212:215], v[60:63], v[152:155]\nds_read_b128 v[208:211], v249 offset:49152\nv_mfma_f32_16x16x32_bf16 v[156:159], v[212:215], v[132:135], v[156:159]\n" \
  "s_waitcnt lgkmcnt(3)\nv_mfma_f32_16x16x32_bf16 v[144:147], v[216:219], v[64:67], v[144:147]\nv_mfma_f32_16x16x32_bf16 v[148:151], v[216:219], v[136:139], v[148:151]\ns_waitcnt lgkmcnt(2)\nv_mfma_f32_16x16x32_bf16 v[152:155], v[220:223], v[64:67], v[152:155]\nv_mfma_f32_16x16x32_bf16 v[156:159], v[220:223], v[136:139], v[156:159]\n" \
  "s_waitcnt lgkmcnt(1)\nv_mfma_f32_16x16x32_bf16 v[144:147], v[224:227], v[68:71], v[144:147]\nv_mfma_f32_16x16x32_bf16 v[148:151], v[224:227], v[140:143], v[148:151]\ns_waitcnt lgkmcnt(0)\nv_mfma_f32_16x16x32_bf16 v[152:155], v[208:211], v[68:71], v[152:155]\nv_mfma_f32_16x16x32_bf16 v[156:159], v[208:211], v[140:143], v[156:159]\n" \
  "s_nop 7\ns_nop 1\n" \
  ""

#define CLOB_FFN "v0", "v1", "v2", "v3", "v4", "v5", "v6", "v7", "v8", "v9", "v10", "v11", "v12", "v13", "v14", "v15", "v16", "v17", "v18", "v19", "v20", "v21", "v22", "v23", "v24", "v25", "v26", "v27", "v28", "v29", "v30", "v31", "v32", "v33", "v34", "v35", "v36", "v37", "v38", "v39", "v40", "v41", "v42", "v43", "v44", "v45", "v46", "v47", "v48", "v49", "v50", "v51", "v52", "v53", "v54", "v55", "v56", "v57", "v58", "v59", "v60", "v61", "v62", "v63", "v64", "v65", "v66", "v67", "v68", "v69", "v70", "v71", "v72", "v73", "v74", "v75", "v76", "v77", "v78", "v79", "v80", "v81", "v82", "v83", "v84", "v85", "v86", "v87", "v88", "v89", "v90", "v91", "v92", "v93", "v94", "v95", "v96", "v97", "v98", "v99", "v100", "v101", "v102", "v103", "v104", "v105", "v106", "v107", "v108", "v109", "v110", "v111", "v112", "v113", "v114", "v115", "v116", "v117", "v118", "v119", "v120", "v121", "v122", "v123", "v124", "v125", "v126", "v127", "v128", "v129", "v130", "v131", "v132", "v133", "v134", "v135", "v136", "v137", "v138", "v139", "v140", "v141", "v142", "v143", "v160", "v161", "v162", "v163", "v164", "v165", "v166", "v167", "v168", "v169", "v170", "v171", "v172", "v173", "v174", "v175", "v176", "v177", "v178", "v179", "v180", "v181", "v182", "v183", "v184", "v185", "v186", "v187", "v188", "v189", "v190", "v191", "v192", "v193", "v194", "v195", "v196", "v197", "v198", "v199", "v200", "v201", "v202", "v203", "v204", "v205", "v206", "v207", "v208", "v209", "v210", "v211", "v212", "v213", "v214", "v215", "v216", "v217", "v218", "v219", "v220", "v221", "v222", "v223", "v224", "v225", "v226", "v227", "v228", "v229", "v230", "v232", "v233", "v234", "v235", "v236", "v237", "v238", "v239", "v240", "v241", "v242", "v243", "v244", "v245", "v246", "v247"
DI void ffn_body(char* __restrict__ ws, u16* __restrict__ out, char* smem, const int b, const int w, const int t) {
  const int xcd = b & 7, idx = b >> 3, a = xcd >> 1;
  const int mt = (xcd & 1) * 4 + (idx & 3), c = idx >> 2, n = a * 8 + c;
  const int m = (xcd & 1) * 8 + (idx >> 2), bb = idx & 3, tg = w & 3, par = w >> 2;
  const unsigned lds0 = (unsigned)(size_t)smem;
  const rsrc_t rx = mk_rsrc(ws + OFF_H2, 2359296), rw = mk_rsrc(ws + OFF_W1, 10616832 + 5308416), rh = mk_rsrc(ws + OFF_HH, 18874368);
  const rsrc_t rp = mk_rsrc(ws + OFF_P2 + (size_t)(m * 4 + bb) * 16 * 9216, 16 * 9216);
  const unsigned w1b = (unsigned)(n * 324 * 1024 + w * 1024), w2b = (unsigned)(10616832 + ((a * 4 + bb) * 324 - 324) * 1024 + w * 1024);
  const unsigned hq = (unsigned)(((mt * 16 + w * 2) * 144 + a * 36) * 1024);
  const int t8 = 2 * tg + par;
  char* cfin = ws + OFF_CNT + (64 + m * 4 + bb) * 128;
  f32x16 co;
  u32x4 vb;
  {
    const unsigned l16 = (t & 63) * 16;
    vb = u32x4{lds0 + l16, lds0 + l16 + 55296, lds0 + l16 + 110592, l16};
    u32x8 sp0 = {lds0 + w * 1024, w1b, w2b, (w < 4 ? w1b : w2b) + 40 * 8192, (unsigned)((mt * 16 + w * 2) * 18 * 1024),
                 (unsigned)(((m * 8 + t8) * 144 + a * 36 + par) * 1024), hq + (8 + (c >> 1)) * 1024 + (c & 1) * 512, hq + c * 1024};
    u32x8 sp1 = {(unsigned)(par * 1024), 0u, 0u, (unsigned)(((m * 8 + (t8 ^ 1)) * 144 + a * 36 + par) * 1024),
                 (unsigned)((((a + 1) & 3) * 4 + a) * 9216 + t8 * 1024), (unsigned)((((a + 2) & 3) * 4 + a) * 9216 + t8 * 1024),
                 (unsigned)((((a + 3) & 3) * 4 + a) * 9216 + t8 * 1024), (unsigned)((tg * 4 + a) * 9216 + 8192 + par * 512)};
    const char* cin = ws + OFF_CNT2 + (m * 4 + a) * 512;
    const char* cout = ws + OFF_CNT2 + ((2 * mt + (w >> 2)) * 4 + a) * 512;
    unsigned v252 = (unsigned)((t & 31) * 16 + ((t >> 5) & 1) * 147456), v253 = lds0 + l16 + 106496 + w * 1024, v254 = lds0 + l16 + 106496 + (w ^ 4) * 1024;
    asm volatile("" :: "{s[40:47]}"(sp0), "{s[48:55]}"(sp1), "s"(cin), "s"(cout), "s"(cfin), "s"(rx), "s"(rh), "s"(rp));
    asm volatile("" : "+v"(vb), "+v"(v252), "+v"(v253), "+v"(v254));
  {
    gu32* fp = (gu32*)(ws + OFF_CNT4 + (mt * 8 + w) * 512) + (t & 3) * 32;
    for (unsigned spin = 0; spin < (1u << 21); ++spin) {
      if (__all(__hip_atomic_load(fp, __ATOMIC_RELAXED, __HIP_MEMORY_SCOPE_AGENT) != 0u)) break;
      __builtin_amdgcn_s_sleep(2);
    }
  }
    asm volatile(FFN_ASM
                 : "={v[144:159]}"(co), "+{v[248:251]}"(vb), "+{s[48:55]}"(sp1)
                 : "{v252}"(v252), "{v253}"(v253), "{v254}"(v254),
                   "{s[40:47]}"(sp0), [rx] "s"(rx), [rw] "s"(rw), [rh] "s"(rh), [rp] "s"(rp), [cfin] "s"(cfin),
                   [cin] "s"(cin), [cout] "s"(cout)
                 : CLOB_FFN, "scc", "memory");
  }
  const int lane = vb[3] >> 4;
  const int ch0 = bb * 144 + 32 * a + 8 * (lane >> 4), tl0 = m * 8 + t8;
  const int ch1 = bb * 144 + 128 + 8 * ((lane >> 4) & 1), tl1 = m * 8 + 2 * a + (lane >> 5);
  const float* gbp = (const float*)(ws + OFF_MODF) + 5 * D_;
  const rsrc_t rx1 = mk_rsrc(ws + OFF_X1, S_ * D_ * 2);
  const u32x4 xv0 = __builtin_amdgcn_raw_buffer_load_b128(rx1, tl0 * 18432 + (ch0 >> 3) * 256 + (lane & 15) * 16, 0, 16);
  const f32x4 g00 = *(const f32x4*)(gbp + ch0), g01 = *(const f32x4*)(gbp + ch0 + 4);
  char* ex = smem + 106496 + lane * 16;
  *(f32x4*)(ex + w * 1024) = f32x4{co[4], co[5], co[6], co[7]}; *(f32x4*)(ex + 8192 + w * 1024) = f32x4{co[12], co[13], co[14], co[15]};
  if (w == 7 && lane == 0) wait_count(cfin, 32u);
  asm volatile("s_waitcnt lgkmcnt(0)\n\ts_barrier" ::: "memory");
  u32x4 pv[3];
#pragma unroll
  for (int q = 0; q < 3; ++q) pv[q] = __builtin_amdgcn_raw_buffer_load_b128(rp, lane * 16, (a * 4 + ((a + 1 + q) & 3)) * 9216 + t8 * 1024, 16);
  if (w == 0) {
    u32x4 pt[4];
#pragma unroll
    for (int q = 0; q < 4; ++q) pt[q] = __builtin_amdgcn_raw_buffer_load_b128(rp, lane * 16, (a * 4 + q) * 9216 + 8192, 16);
    const u32x4 xv1 = __builtin_amdgcn_raw_buffer_load_b128(rx1, tl1 * 18432 + (ch1 >> 3) * 256 + (lane & 15) * 16, 0, 16);
    const f32x4 g10 = *(const f32x4*)(gbp + ch1), g11 = *(const f32x4*)(gbp + ch1 + 4);
    u32x4 o;
#pragma unroll
    for (int k = 0; k < 4; ++k)
      o[k] = pk2(blo(xv1[k]) + (k < 2 ? g10 : g11)[(2 * k) & 3] * ((blo(pt[0][k]) + blo(pt[1][k])) + (blo(pt[2][k]) + blo(pt[3][k]))),
                 bhi(xv1[k]) + (k < 2 ? g10 : g11)[(2 * k + 1) & 3] * ((bhi(pt[0][k]) + bhi(pt[1][k])) + (bhi(pt[2][k]) + bhi(pt[3][k]))));
    *(u32x4*)(out + (size_t)(tl1 * 16 + (lane & 15)) * D_ + ch1) = o;
  }
  {
    const f32x4 ev = f32x4{co[0], co[1], co[2], co[3]} + *(const f32x4*)(ex + (w ^ 4) * 1024);
    const f32x4 od = f32x4{co[8], co[9], co[10], co[11]} + *(const f32x4*)(ex + 8192 + (w ^ 4) * 1024);
    u32x4 o;
#pragma unroll
    for (int k = 0; k < 4; ++k)
      o[k] = pk2(blo(xv0[k]) + (k < 2 ? g00 : g01)[(2 * k) & 3] * ((blo(pv[0][k]) + blo(pv[1][k])) + (blo(pv[2][k]) + ev[k])),
                 bhi(xv0[k]) + (k < 2 ? g00 : g01)[(2 * k + 1) & 3] * ((bhi(pv[0][k]) + bhi(pv[1][k])) + (bhi(pv[2][k]) + od[k])));
    *(u32x4*)(out + (size_t)(tl0 * 16 + (lane & 15)) * D_ + ch0) = o;
  }
}

extern "C" __global__ __launch_bounds__(512, 2) void k_main(char* __restrict__ ws, const u16* __restrict__ x, const u16* __restrict__ wg, const u16* __restrict__ wu, const u16* __restrict__ wd, u16* __restrict__ out) {
  extern __shared__ __attribute__((aligned(16))) char smem[];
  const int b = blockIdx.x, t = threadIdx.x, lane0 = t & 63, w = rfl(t >> 6);
  const int tt = (b & 7) * 8 + (b >> 5), g = (b >> 3) & 3;
  const int hp = w & 3, kh = w >> 2, headA = g * 8 + 2 * hp;
  const int r0 = lane0 & 31, h0 = lane0 >> 5;
  const unsigned lds0 = (unsigned)(size_t)smem, l16 = lane0 * 16, kvs = w * 1024;
  const rsrc_t rq = mk_rsrc(ws + OFF_QM, 2097152 + 262144), rkv = mk_rsrc(ws + OFF_KV, KV_BYTES), rwo = mk_rsrc(ws + OFF_WO, 663552);
  const unsigned wo0 = (unsigned)((g * 18 + w) * 9 * 1024), wo1 = wo0 + 8 * 9 * 1024, wo2 = (unsigned)((g * 18 + 16) * 9 * 1024) + kvs;
  const unsigned kvlast = w < 6 ? kvs + 18 * 8192 : 149 * 1024;
  unsigned qt1; u32x4 qa4, qb4;
  dma16(rkv, l16, kvs, lds0 + kvs);
  ld16<0>(qa4, rq, l16, (unsigned)((tt * 32 + headA) * 1024));
  ld16<1024>(qb4, rq, l16, (unsigned)((tt * 32 + headA) * 1024));
  asm volatile("buffer_load_dword %0, %1, %2, %3 offen" : "=v"(qt1) : "v"((unsigned)(lane0 * 4)), "s"(rq), "s"((unsigned)(2097152 + (tt * 32 + headA) * 128)) : "memory");
  dma16(rkv, l16, kvs + 8192, lds0 + kvs + 8192);
  dma16(rkv, l16, kvs + 16384, lds0 + kvs + 16384);
  const int task = b * 8 + w;
  const RetileTask rt_ = retile_task(task, lane0);
  const rsrc_t rsrc = mk_rsrc(task < 648 ? wg : task < 1296 ? wu : wd, D_ * F_ * 2);
  const rsrc_t rdst = mk_rsrc(ws + OFF_W1, (unsigned)(WS_END - OFF_W1));
  const rsrc_t rxin = mk_rsrc(x, S_ * D_ * 2), rmx = mk_rsrc(w < 7 ? (const void*)(ws + OFF_MODF) : (const void*)x, w < 7 ? 3456 * 4 * 2 : S_ * D_ * 2);
  const unsigned xso = (unsigned)((tt * 32 + g * 8) * (D_ * 2) + w * 1024);
  CODE_RSRC(rcode, "k_main", "k_text_end");
  asm volatile("s_waitcnt vmcnt(0)\n\ts_barrier" : "+v"(qa4), "+v"(qb4), "+v"(qt1) :: "memory");
  const int voff0 = 1152 + (r0 < 19 ? (h0 * 19 + r0) * 16 : 38 * 16);
  f32x32 oab; u32x32 wfa, wfb; u32x8 wfc, misc;
  {
    const f32x32 z = {};
    const char* kb = smem + kh * KV_TILE;
    const bf16x8 k0 = *(const bf16x8*)(smem + lane0 * 16), k1 = *(const bf16x8*)(kb + lane0 * 16);
    const unsigned kt0 = *(const unsigned*)(kb + 1024 + r0 * 4), kt1 = *(const unsigned*)(kb + 2 * KV_TILE + 1024 + r0 * 4);
    const f32x16 z16 = {};
    const f32x16 ta = MFMA32(k0, BC8(qa4), z16), tb = MFMA32(k0, BC8(qb4), z16);
    const f32x16 ua = MFMA32(k1, BC8(qa4), z16), ub = MFMA32(k1, BC8(qb4), z16);
    const auto msw = __builtin_amdgcn_permlane32_swap(__float_as_uint(max16q(ta)), __float_as_uint(max16q(tb)), false, false);
    const u32x2 qt2 = {qt1, pk2(-60.f - fmaxf(__uint_as_float(msw[0]), __uint_as_float(msw[1])), 0.f)};
    const s16x4 qts = __builtin_bit_cast(s16x4, qt2);
    f32x32 s0 = __builtin_amdgcn_mfma_f32_32x32x4bf16_1k(__builtin_bit_cast(s16x4, (u32x2{kt0, 0x3f80u})), qts, CAT16(ua, ub), 0, 0, 0);
    f32x32 s1 = __builtin_amdgcn_mfma_f32_32x32x4bf16_1k(__builtin_bit_cast(s16x4, (u32x2{kt1, 0x3f80u})), qts, z, 0, 0, 0);
    u32x4 km = *(const u32x4*)(kb + 2 * KV_TILE + lane0 * 16), v0 = *(const u32x4*)(kb + voff0);
    u32x2 kt = {*(const unsigned*)(kb + 4 * KV_TILE + 1024 + r0 * 4), 0x3f80u};
    misc = u32x8{qt2[0], qt2[1], lds0 + kh * KV_TILE + lane0 * 16, lds0 + kh * KV_TILE + 1024 + r0 * 4, lds0 + kh * KV_TILE + voff0, l16, rt_.vsrc, rt_.vlo};
    u32x8 sp0 = {kvs, lds0 + kvs, wo0, wo1, wo2, kvlast, lds0 + kvlast, 0u}, sp1 = {(unsigned)rfl(rt_.srow), (unsigned)rfl(rt_.ld), (unsigned)rfl(rt_.jump), 0x05040100u, 0x07060302u,
                                                                                   (unsigned)(((g * 18 + 16) * 9 + 16 + (w & 1)) * 1024), lds0 + 32768 + (16 + (w & 1)) * 1024, xso};
    unsigned acq = w ^ 3;
    u32x2 sp2 = {lds0 + (w < 7 ? 62464 + w * 1024 : 61440), w < 7 ? 2 * D_ * 4 + w * 1024 : xso + 1024};
    asm volatile("s_nop 7\n\ts_nop 7\n\ts_nop 3\n\t" ATTN_ASM
                 : "={v[0:31]}"(oab), "+{v[32:63]}"(s0), "+{v[64:95]}"(s1), "+{v[106:109]}"(km), "+{v[110:113]}"(v0), "+{v[126:127]}"(kt),
                   "+{v[128:135]}"(misc), "={v[136:167]}"(wfa), "={v[168:199]}"(wfb), "={v[200:207]}"(wfc),
                   "+{s[40:47]}"(sp0), "+{s[48:55]}"(sp1), "+{s58}"(acq)
                 : "{v[118:121]}"(qa4), "{v[122:125]}"(qb4), "{v244}"(rt_.vhi), "{v245}"(l16 + (unsigned)(((b >> 3) + 32 * w) * 1024)), "{s[56:57]}"(sp2),
                   [rkv] "s"(rkv), [rwo] "s"(rwo), [rsrc] "s"(rsrc), [rdst] "s"(rdst), [rxin] "s"(rxin), [rmx] "s"(rmx), [rcode] "s"(rcode), [wcnt] "s"(ws + OFF_CNT + ((b & 7) * 8 + w) * 128), [wall] "s"(ws + OFF_CNT)
                 : "v96", "v97", "v98", "v99", "v100", "v101", "v102", "v103", "v104", "v105", "v114", "v115", "v116", "v117", CLOB_208_243, "vcc", "scc", "memory");
  }
  const int lane = misc[5] >> 4, r = lane & 31, h = lane >> 5, voff = 1152 + (r < 19 ? (h * 19 + r) * 16 : 38 * 16);
  const f32x16 oa = LO16(oab), ob = HI16(oab);
  {
    const f32x16 snd = kh ? oa : ob;
    char* ex = smem + w * 3072 + lane * 16;
    *(f32x4*)ex = f32x4{snd[0], snd[1], snd[2], snd[3]}; *(f32x4*)(ex + 1024) = f32x4{snd[4], snd[5], snd[6], snd[7]}; *(f32x4*)(ex + 2048) = f32x4{snd[8], snd[9], snd[10], snd[11]};
  }
  asm volatile("s_waitcnt lgkmcnt(0)\n\ts_barrier" ::: "memory");
  f32x16 o = kh ? ob : oa;
  {
    const char* ey = smem + (w ^ 4) * 3072 + lane * 16;
    const f32x4 e0 = *(const f32x4*)ey, e1 = *(const f32x4*)(ey + 1024), e2 = *(const f32x4*)(ey + 2048);
#pragma unroll
    for (int i = 0; i < 4; ++i) { o[i] += e0[i]; o[4 + i] += e1[i]; o[8 + i] += e2[i]; }
  }
  const int hl = 2 * hp + kh;
  float l;
  {
    auto pr = __builtin_amdgcn_permlane32_swap(__float_as_uint(o[10]), __float_as_uint(o[10]), false, false); l = __uint_as_float(pr[0]);
    float am = fabsf(o[0]), sm = o[0];
#pragma unroll
    for (int i = 1; i < 10; ++i) { am = fmaxf(am, fabsf(o[i])); sm += o[i]; }
    const bool ok = (l < 1.2980742e33f) && (l > 8.0779357e-28f) && (am < 3.0e38f) && (sm == sm);
    if (!__all(ok)) {
      const char* gk = ws + OFF_KV;
      const bf16x8 qm = BC8(kh ? qb4 : qa4);
      const unsigned qtl = *(const unsigned*)(ws + OFF_QT + (size_t)(tt * 32 + g * 8 + hl) * 128 + r * 4);
      const s16x4 qt = __builtin_bit_cast(s16x4, (u32x2{h ? 0u : qtl, 0u}));
      float mm = -3.4e38f;
      for (int tl = 0; tl < 64; ++tl) { f32x16 z = {}; mm = fmaxf(mm, max16(qk_tile(gk + tl * KV_TILE + lane * 16, gk + tl * KV_TILE + 1024 + r * 4, qm, qt, z))); }
      mm = half_swap_max(mm);
      f32x16 negm;
#pragma unroll
      for (int i = 0; i < 16; ++i) negm[i] = -mm;
      o = f32x16{};
      for (int tl = 0; tl < 64; ++tl) {
        const f32x16 sc = qk_tile(gk + tl * KV_TILE + lane * 16, gk + tl * KV_TILE + 1024 + r * 4, qm, qt, negm);
        u32x4 p0, p1;
#pragma unroll
        for (int e = 0; e < 4; ++e) { p0[e] = pk2(__builtin_amdgcn_exp2f(sc[2 * e]), __builtin_amdgcn_exp2f(sc[2 * e + 1])); p1[e] = pk2(__builtin_amdgcn_exp2f(sc[8 + 2 * e]), __builtin_amdgcn_exp2f(sc[8 + 2 * e + 1])); }
        o = MFMA32(*(const bf16x8*)(gk + tl * KV_TILE + voff), BC8(p0), o);
        o = MFMA32(*(const bf16x8*)(gk + tl * KV_TILE + voff + 624), BC8(p1), o);
      }
      auto p2 = __builtin_amdgcn_permlane32_swap(__float_as_uint(o[10]), __float_as_uint(o[10]), false, false); l = __uint_as_float(p2[0]);
    }
  }
  const float inv = 1.f / l;
  char* ox = smem + KV_BYTES;
  *(u32x4*)(ox + hl * 1024 + lane * 16) = u32x4{pk2(o[0] * inv, o[1] * inv), pk2(o[2] * inv, o[3] * inv), pk2(o[4] * inv, o[5] * inv), pk2(o[6] * inv, o[7] * inv)};
  if (h == 0) *(unsigned*)(ox + 8192 + ((hl >> 2) * 32 + r) * 16 + (hl & 3) * 4) = pk2(o[8] * inv, o[9] * inv);
  asm volatile("s_waitcnt lgkmcnt(0)\n\ts_barrier" ::: "memory");
  u32x4 wf[18];
#pragma unroll
  for (int i = 0; i < 8; ++i) {
    wf[i] = u32x4{wfa[4 * i], wfa[4 * i + 1], wfa[4 * i + 2], wfa[4 * i + 3]};
    wf[8 + i] = u32x4{wfb[4 * i], wfb[4 * i + 1], wfb[4 * i + 2], wfb[4 * i + 3]};
  }
  wf[16] = u32x4{wfc[0], wfc[1], wfc[2], wfc[3]}; wf[17] = u32x4{wfc[4], wfc[5], wfc[6], wfc[7]};
  bf16x8 of[9];
#pragma unroll
  for (int k = 0; k < 9; ++k) of[k] = *(const bf16x8*)(ox + k * 1024 + lane * 16);
  const rsrc_t rop = mk_rsrc(ws + OFF_OP + (size_t)tt * 4 * 36 * 1024, 4 * 36 * 1024);
  const bool own = (r >> 3) == g;
  char* ownp = smem + 1024 + h * 128 + (r & 7) * 16;
#pragma unroll
  for (int j = 0; j < 3; ++j) {
    if (j == 2 && w >= 2) break;
    f32x16 acc = {};
#pragma unroll
    for (int k = 0; k < 9; ++k) acc = MFMA32(j < 2 ? BC8(wf[9 * j + k]) : *(const bf16x8*)(smem + 32768 + (w * 9 + k) * 1024 + lane * 16), of[k], acc);
    const int rt = j < 2 ? w + 8 * j : 16 + w;
    const u32x4 p0 = {pk2(acc[0], acc[1]), pk2(acc[2], acc[3]), pk2(acc[4], acc[5]), pk2(acc[6], acc[7])};
    const u32x4 p1 = {pk2(acc[8], acc[9]), pk2(acc[10], acc[11]), pk2(acc[12], acc[13]), pk2(acc[14], acc[15])};
    __builtin_amdgcn_raw_buffer_store_b128(p0, rop, (own ? 0x80000000u : lane * 16) + (g * 36 + rt * 2) * 1024, 0, 16);
    __builtin_amdgcn_raw_buffer_store_b128(p1, rop, (own ? 0x80000000u : lane * 16) + (g * 36 + rt * 2 + 1) * 1024, 0, 16);
    if (own) { *(u32x4*)(ownp + rt * 512) = p0; *(u32x4*)(ownp + rt * 512 + 256) = p1; }
  }
  asm volatile("s_waitcnt vmcnt(0) lgkmcnt(0)\n\ts_barrier" ::: "memory");
  gu32* flg = (gu32*)(ws + OFF_CNT3 + tt * 512);
  if (w == 7 && lane == 0) __hip_atomic_store(flg + g * 32, 1u, __ATOMIC_RELAXED, __HIP_MEMORY_SCOPE_AGENT);
  const int tk = lane & 7, ci = lane >> 3, tk8 = g * 8 + tk, tok = tt * 32 + tk8, c8 = 8 * w + ci, ce = 512 + c8;
  const u32x4 xv = *(const u32x4*)(smem + 53248 + tk * (D_ * 2) + c8 * 16), po = *(const u32x4*)(smem + 1024 + c8 * 128 + tk * 16);
  const unsigned xe = *(const u16*)(smem + 53248 + tk * (D_ * 2) + ce * 2), pe = *(const u16*)(smem + 1024 + (64 + w) * 128 + tk * 16 + ci * 2);
  f32x4 mv[3][2]; float me[3];
#pragma unroll
  for (int k = 0; k < 3; ++k) {
    mv[k][0] = *(const f32x4*)(smem + 62464 + k * D_ * 4 + c8 * 32); mv[k][1] = *(const f32x4*)(smem + 62464 + k * D_ * 4 + c8 * 32 + 16);
    me[k] = *(const float*)(smem + 62464 + k * D_ * 4 + ce * 4);
  }
  float v[9];
#pragma unroll
  for (int e = 0; e < 4; ++e) { v[2 * e] = blo(xv[e]) + mv[0][e >> 1][(2 * e) & 3] * blo(po[e]); v[2 * e + 1] = bhi(xv[e]) + mv[0][e >> 1][(2 * e + 1) & 3] * bhi(po[e]); }
  v[8] = blo(xe) + me[0] * blo(pe);
  mv[2][0] += 1.f; mv[2][1] += 1.f; me[2] += 1.f;
  unsigned pa = c8 * 512 + tk8 * 16, pb = (64 + w) * 512 + tk8 * 16 + (ci >> 1) * 4, psh = (ci & 1) * 16;
  unsigned fo = (unsigned)((tok >> 4) * 18432 + c8 * 256 + (tok & 15) * 16), fe = lane < 8 ? (unsigned)((tok >> 4) * 18432 + (64 + w) * 256 + (tok & 15) * 16) : 0x80000000u;
  asm volatile("" : "+v"(v[0]), "+v"(v[1]), "+v"(v[2]), "+v"(v[3]), "+v"(v[4]), "+v"(v[5]), "+v"(v[6]), "+v"(v[7]), "+v"(v[8]), "+v"(mv[2][0]), "+v"(mv[2][1]), "+v"(me[2]),
               "+v"(mv[1][0]), "+v"(mv[1][1]), "+v"(me[1]), "+v"(pa), "+v"(pb), "+v"(psh), "+v"(fo), "+v"(fe));
  if (w == 6) {
    gu32* fp = flg + (lane & 3) * 32;
    for (unsigned spin = 0; spin < (1u << 21); ++spin) {
      if (__all(__hip_atomic_load(fp, __ATOMIC_RELAXED, __HIP_MEMORY_SCOPE_AGENT) != 0u || (lane & 3) == g)) break;
      __builtin_amdgcn_s_sleep(1);
    }
  }
  asm volatile("s_barrier" ::: "memory");
  u32x4 pv[3]; unsigned pq[3];
#pragma unroll
  for (int q = 0; q < 3; ++q) pv[q] = __builtin_amdgcn_raw_buffer_load_b128(rop, pa, ((g + 1 + q) & 3) * 36 * 1024, 16);
#pragma unroll
  for (int q = 0; q < 3; ++q) pq[q] = __builtin_amdgcn_raw_buffer_load_b32(rop, pb, ((g + 1 + q) & 3) * 36 * 1024, 16);
#pragma unroll
  for (int e = 0; e < 4; ++e) {
    v[2 * e]     += mv[0][e >> 1][(2 * e) & 3] * (blo(pv[0][e]) + blo(pv[1][e]) + blo(pv[2][e]));
    v[2 * e + 1] += mv[0][e >> 1][(2 * e + 1) & 3] * (bhi(pv[0][e]) + bhi(pv[1][e]) + bhi(pv[2][e]));
  }
  v[8] += me[0] * (blo(pq[0] >> psh) + blo(pq[1] >> psh) + blo(pq[2] >> psh));
  const rsrc_t rw = mk_rsrc(ws + OFF_W1, 10616832 + 5308416);
  const unsigned w1b = (unsigned)(((((b & 7) >> 1) * 8 + (b >> 5)) * 324 + w) * 1024);
  asm volatile("" : "+v"(v[0]), "+v"(v[7]), "+v"(v[8]));
#pragma unroll
  for (int i = 0; i < FFN_PRE0; ++i) dma16s(rw, lane * 16, w1b + i * 8192, lds0 + (32 + i * 8 + w) * 1024);
  const float ml = (((v[0] + v[1]) + (v[2] + v[3])) + ((v[4] + v[5]) + (v[6] + v[7])) + v[8]) * (1.f / 9.f);
  float q2 = 0.f;
#pragma unroll
  for (int e = 0; e < 9; ++e) { const float d = v[e] - ml; q2 += d * d; }
  const float mw = xsum8_16_32(ml) * 0.125f;
  q2 = xsum8_16_32(q2 + 9.f * (ml - mw) * (ml - mw));
  float* red = (float*)smem;
  if (lane < 8) *(f32x2*)(red + (lane * 8 + w) * 2) = f32x2{mw, q2};
  const rsrc_t rx1 = mk_rsrc(ws + OFF_X1, S_ * D_ * 2), rh2 = mk_rsrc(ws + OFF_H2, 2359296);
  asm volatile("s_waitcnt lgkmcnt(0)\n\ts_barrier" ::: "memory");
  float mean = 0.f, m2 = 0.f;
  {
    f32x4 t4[4];
#pragma unroll
    for (int k = 0; k < 4; ++k) t4[k] = *(const f32x4*)(red + tk * 16 + k * 4);
    __builtin_amdgcn_raw_buffer_store_b128(u32x4{pk2(v[0], v[1]), pk2(v[2], v[3]), pk2(v[4], v[5]), pk2(v[6], v[7])}, rx1, fo, 0, 16);
    __builtin_amdgcn_raw_buffer_store_b128(gather8(v[8]), rx1, fe, 0, 16);
#pragma unroll
    for (int k = 0; k < 4; ++k) mean += t4[k][0] + t4[k][2];
    mean *= 0.125f;
#pragma unroll
    for (int k = 0; k < 4; ++k) { const float d0 = t4[k][0] - mean, d1 = t4[k][2] - mean; m2 += t4[k][1] + t4[k][3] + 72.f * (d0 * d0 + d1 * d1); }
  }
  const float rs = __builtin_amdgcn_rsqf(m2 * (1.f / D_) + 1e-5f);
  u32x4 ho;
#pragma unroll
  for (int e = 0; e < 4; ++e)
    ho[e] = pk2((v[2 * e] - mean) * rs * mv[2][e >> 1][(2 * e) & 3] + mv[1][e >> 1][(2 * e) & 3],
                (v[2 * e + 1] - mean) * rs * mv[2][e >> 1][(2 * e + 1) & 3] + mv[1][e >> 1][(2 * e + 1) & 3]);
  __builtin_amdgcn_raw_buffer_store_b128(ho, rh2, fo, 0, 16);
  __builtin_amdgcn_raw_buffer_store_b128(gather8((v[8] - mean) * rs * me[2] + me[1]), rh2, fe, 0, 16);
  asm volatile("s_waitcnt vmcnt(0)\n\ts_barrier" ::: "memory");
  if (w == 7 && lane == 0) __hip_atomic_store((gu32*)(ws + OFF_CNT4 + tt * 512) + g * 32, 1u, __ATOMIC_RELAXED, __HIP_MEMORY_SCOPE_AGENT);
  {
#pragma unroll
    for (int i = FFN_PRE0; i < FFN_PRER; ++i) dma16s(rw, lane * 16, w1b + i * 8192, lds0 + (32 + i * 8 + w) * 1024);
  }
  ffn_body(ws, out, smem, b, w, lane);
}

extern "C" __global__ void k_text_end() {}

extern "C" void kernel_launch(void* const* d_in, const int* in_sizes, int n_in, void* d_out, int out_size, void* d_ws, size_t ws_size,
                              hipStream_t stream) {
  const u16 *x = (const u16*)d_in[0], *wq = (const u16*)d_in[1], *wk = (const u16*)d_in[2], *wv = (const u16*)d_in[3], *wo = (const u16*)d_in[4];
  const u16 *wg = (const u16*)d_in[6], *wu = (const u16*)d_in[7], *wd = (const u16*)d_in[8], *c = (const u16*)d_in[9], *wm = (const u16*)d_in[10];
  char* ws = (char*)d_ws;
  (void)hipFuncSetAttribute((const void*)k_main, hipFuncAttributeMaxDynamicSharedMemorySize, 163840);
  k_prep<<<256, 896, 0, stream>>>(x, wq, wk, wv, wo, c, wm, ws);
  k_qkv<<<256, 704, 0, stream>>>(ws);
  k_main<<<256, 512, 163840, stream>>>(ws, x, wg, wu, wd, (u16*)d_out);
}
```
